```python
import torch

def _softmax(x, axis=-1):
    return torch.softmax(x, dim=axis)
def _gelu(x):
    return torch.nn.functional.gelu(x, approximate="tanh")
def _silu(x):
    return torch.nn.functional.silu(x)
def _sigmoid(x):
    return torch.sigmoid(x)
def _layernorm(x, axis=-1, eps=1e-5):
    m = x.mean(dim=axis, keepdim=True)
    v = x.var(dim=axis, keepdim=True, unbiased=False)
    return (x - m) / torch.sqrt(v + eps)
def _scatter_add(tgt, idx, src, axis=0):
    return tgt.clone().index_add_(axis, idx, src)
def _segment_sum(src, seg, n):
    out = torch.zeros((n,) + src.shape[1:], dtype=src.dtype)
    return out.index_add_(0, seg, src)
def _pad(x, pw):
    flat = tuple(v for pair in reversed(pw) for v in pair)
    return torch.nn.functional.pad(x, flat)

kernel_name = 'standalone__gqa_block_cached__near_ridge'

def setup_inputs():

    _w = (lambda a: (lambda t: t.to(torch.bfloat16) if t.is_floating_point() else t)(torch.as_tensor(a)))
    B, Sq, D = 8, 64, 512
    Hq, Hkv, Sk, Dh = 32, 8, 512, 128
    assert Hq % Hkv == 0, (Hq, Hkv)
    return {
        "x": _w((lambda *s: torch.randn(*s, dtype=torch.bfloat16) * 0.8)(B, Sq, D)),
        "k_cache": _w((lambda *s: torch.randn(*s, dtype=torch.bfloat16) * 0.8)(B, Hkv, Sk, Dh)),
        "v_cache": _w((lambda *s: torch.randn(*s, dtype=torch.bfloat16) * 0.05)(B, Hkv, Sk, Dh)),
        "wq": _w((lambda *s: torch.randn(*s, dtype=torch.bfloat16) * 0.8)(D, Hq * Dh) * (D ** -0.5)),
        "wo": _w((lambda *s: torch.randn(*s, dtype=torch.bfloat16) * 0.05)(Hq * Dh, D) * ((Hq * Dh) ** -0.5)),
    }

def reference(x, k_cache, v_cache, wq, wo):
    B, Sq, D = x.shape
    Hq, Dh = 32, 128
    Hkv = k_cache.shape[1]
    rep = Hq // Hkv
    q = (lambda x, *d: x.permute(*d))((x @ wq).reshape(B, Sq, Hq, Dh), 0, 2, 1, 3)
    k = (lambda x, n, axis: torch.repeat_interleave(x, n, dim=axis))(k_cache, rep, axis=1)
    v = (lambda x, n, axis: torch.repeat_interleave(x, n, dim=axis))(v_cache, rep, axis=1)
    scores = torch.einsum("bhqd,bhkd->bhqk", q, k) * (Dh ** -0.5)
    p = _softmax(scores, axis=-1)
    o = torch.einsum("bhqk,bhkd->bhqd", p, v)
    return (lambda x, *d: x.permute(*d))(o, 0, 2, 1, 3).reshape(B, Sq, Hq * Dh) @ wo
```

```cpp
#include <hip/hip_runtime.h>
#include <hip/hip_bf16.h>
typedef __hip_bfloat16 bf16;
constexpr int B=8,SQ=64,DM=512,HQ=32,HKV=8,SK=512,DH=128;
__device__ __forceinline__ float b2f(bf16 v){return __bfloat162float(v);}
__global__ void k_q(const bf16* x,const bf16* wq,bf16* q){
  int n=blockIdx.x*256+threadIdx.x; int m=blockIdx.y;
  float a=0; for(int k=0;k<DM;++k) a+=b2f(x[m*DM+k])*b2f(wq[(size_t)k*HQ*DH+n]);
  q[(size_t)m*HQ*DH+n]=__float2bfloat16(a);
}
__global__ void k_attn(const bf16* q,const bf16* K,const bf16* V,bf16* o){
  int s=blockIdx.x,h=blockIdx.y,b=blockIdx.z; int kvh=h/(HQ/HKV);
  __shared__ float p[SK]; __shared__ float qs[DH]; __shared__ float red[256];
  int t=threadIdx.x;
  if(t<DH) qs[t]=b2f(q[(size_t)(b*SQ+s)*HQ*DH+h*DH+t]);
  __syncthreads();
  const bf16* Kb=K+(size_t)(b*HKV+kvh)*SK*DH; const bf16* Vb=V+(size_t)(b*HKV+kvh)*SK*DH;
  float mx=-1e30f;
  for(int j=t;j<SK;j+=256){ float a=0; for(int d=0;d<DH;++d) a+=qs[d]*b2f(Kb[j*DH+d]); a*=0.08838834764831845f; p[j]=a; mx=fmaxf(mx,a);}
  red[t]=mx; __syncthreads();
  for(int o2=128;o2>0;o2>>=1){ if(t<o2) red[t]=fmaxf(red[t],red[t+o2]); __syncthreads(); }
  mx=red[0]; __syncthreads();
  float sm=0; for(int j=t;j<SK;j+=256){ float e=__expf(p[j]-mx); p[j]=e; sm+=e;}
  red[t]=sm; __syncthreads();
  for(int o2=128;o2>0;o2>>=1){ if(t<o2) red[t]+=red[t+o2]; __syncthreads(); }
  sm=red[0];
  if(t<DH){ float a=0; for(int j=0;j<SK;++j) a+=p[j]*b2f(Vb[j*DH+t]); o[(size_t)(b*SQ+s)*HQ*DH+h*DH+t]=__float2bfloat16(a/sm);}
}
__global__ void k_o(const bf16* o,const bf16* wo,bf16* out){
  int n=blockIdx.x*256+threadIdx.x; int m=blockIdx.y;
  float a=0; for(int k=0;k<HQ*DH;++k) a+=b2f(o[(size_t)m*HQ*DH+k])*b2f(wo[(size_t)k*DM+n]);
  out[(size_t)m*DM+n]=__float2bfloat16(a);
}
extern "C" void kernel_launch(void* const* d_in, const int* in_sizes, int n_in, void* d_out, int out_size, void* d_ws, size_t ws_size, hipStream_t stream) {
  const bf16* x=(const bf16*)d_in[0]; const bf16* K=(const bf16*)d_in[1]; const bf16* V=(const bf16*)d_in[2]; const bf16* wq=(const bf16*)d_in[3]; const bf16* wo=(const bf16*)d_in[4];
  bf16* q=(bf16*)d_ws; bf16* o=q+(size_t)B*SQ*HQ*DH;
  k_q<<<dim3(HQ*DH/256,B*SQ),256,0,stream>>>(x,wq,q);
  k_attn<<<dim3(SQ,HQ,B),256,0,stream>>>(q,K,V,o);
  k_o<<<dim3(DM/256,B*SQ),256,0,stream>>>(o,wo,(bf16*)d_out);
}
```

```cpp
#include <hip/hip_runtime.h>
#include <hip/hip_bf16.h>

typedef __attribute__((ext_vector_type(8))) __bf16 bf16x8;
typedef __attribute__((ext_vector_type(2))) __bf16 bf16x2;
typedef __attribute__((ext_vector_type(4))) short s16x4;
typedef __attribute__((ext_vector_type(8))) short s16x8;
typedef __attribute__((ext_vector_type(16))) float f32x16;
typedef __attribute__((ext_vector_type(4))) float f32x4;
typedef __attribute__((ext_vector_type(4))) unsigned u32x4;
typedef __attribute__((ext_vector_type(2))) unsigned u32x2;
#define LAS __attribute__((address_space(3)))
#define DEV __device__ __forceinline__

__device__ unsigned g_flag1[256 * 32];
__device__ unsigned g_flag2[256 * 32];

#define ST(i)

constexpr unsigned H = 8192;
#define BAR() asm volatile("s_barrier" ::: "memory")
#define BAR_C() asm volatile("s_waitcnt lgkmcnt(0)\n\ts_barrier" ::: "memory")
#define BAR_L(n) asm volatile("s_waitcnt vmcnt(%0)\n\ts_barrier" ::"i"(n) : "memory")
#define SB() __builtin_amdgcn_sched_barrier(0)

DEV void dma(unsigned m0v, unsigned voff, __amdgpu_buffer_rsrc_t srd, unsigned soff) {
  asm volatile("s_mov_b32 m0, %0\n\ts_nop 0\n\tbuffer_load_dwordx4 %1, %2, %3 offen lds" ::"s"(m0v), "v"(voff), "s"(srd), "s"(soff) : "memory");
}
DEV void dma_sc1(unsigned m0v, unsigned voff, __amdgpu_buffer_rsrc_t srd, unsigned soff) {
  asm volatile("s_mov_b32 m0, %0\n\ts_nop 0\n\tbuffer_load_dwordx4 %1, %2, %3 offen sc1 lds" ::"s"(m0v), "v"(voff), "s"(srd), "s"(soff) : "memory");
}
DEV bf16x8 ld128(LAS char* p) { return *(LAS bf16x8*)p; }
DEV bf16x8 ldtr(LAS char* p, unsigned tstep) {
  s16x4 a = __builtin_amdgcn_ds_read_tr16_b64_v4i16((LAS s16x4*)p);
  s16x4 b = __builtin_amdgcn_ds_read_tr16_b64_v4i16((LAS s16x4*)(p + tstep));
  s16x8 r = __builtin_shufflevector(a, b, 0, 1, 2, 3, 4, 5, 6, 7);
  return __builtin_bit_cast(bf16x8, r);
}
DEV unsigned pk(float a, float b) { bf16x2 r = {(__bf16)a, (__bf16)b}; return __builtin_bit_cast(unsigned, r); }
DEV f32x16 mfma(bf16x8 a, bf16x8 b, f32x16 c) { return __builtin_amdgcn_mfma_f32_32x32x16_bf16(a, b, c, 0, 0, 0); }

constexpr int KH[4][4] = {{15, 16, 17, 18}, {19, 9, 10, 11}, {12, 13, 14, 0}, {3, 4, 5, 6}};
constexpr int VH[4][4] = {{7, 8, 1, 2}, {15, 16, 17, 18}, {19, 9, 10, 11}, {12, 13, 14, 0}};
constexpr int WH[4][4] = {{7, 8, 1, 2}, {15, 16, 17, 19}, {11, 14, 0, 18}, {7, 8, 1, 2}};
constexpr int OH[4] = {9, 12, 3, 5};
constexpr int QH = 1, SUMH = 18, PH = 3;
constexpr unsigned pack4(const int* a) { return a[0] | (a[1] << 8) | (a[2] << 16) | (a[3] << 24); }

struct Frag { bf16x8 a, b0, b1; };

__global__ __launch_bounds__(512, 2) void gqa_fused(const void* __restrict__ x, const void* __restrict__ kc, const void* __restrict__ vc,
                                                    const void* __restrict__ wq, const void* __restrict__ wo, void* __restrict__ out, char* __restrict__ ws) {
  extern __shared__ __attribute__((aligned(16))) char smem[];
  LAS char* lds = (LAS char*)smem;
  const unsigned ldsb = (unsigned)(size_t)lds;
  const int wid = blockIdx.x, kvh = wid & 7, g = (wid >> 3) & 3, b = wid >> 5, h = kvh * 4 + g;
  const int wave = __builtin_amdgcn_readfirstlane(threadIdx.x >> 6), lane = threadIdx.x & 63;
  const __amdgpu_buffer_rsrc_t sO = __builtin_amdgcn_make_buffer_rsrc(ws, 0, 4u << 20, 0x00020000);
  const __amdgpu_buffer_rsrc_t sP = __builtin_amdgcn_make_buffer_rsrc(ws + (4u << 20), 0, 8u << 20, 0x00020000);
  ST(0);
  if (wave >= 4) {
    const int lw = wave - 4;
    const __amdgpu_buffer_rsrc_t sX = __builtin_amdgcn_make_buffer_rsrc((void*)x, 0, 8u * 64 * 512 * 2, 0x00020000);
    const __amdgpu_buffer_rsrc_t sK = __builtin_amdgcn_make_buffer_rsrc((void*)kc, 0, 8u << 20, 0x00020000);
    const __amdgpu_buffer_rsrc_t sV = __builtin_amdgcn_make_buffer_rsrc((void*)vc, 0, 8u << 20, 0x00020000);
    const __amdgpu_buffer_rsrc_t sWq = __builtin_amdgcn_make_buffer_rsrc((void*)wq, 0, 4u << 20, 0x00020000);
    const __amdgpu_buffer_rsrc_t sWo = __builtin_amdgcn_make_buffer_rsrc((void*)wo, 0, 4u << 20, 0x00020000);
    const unsigned r4 = lane >> 4, c16 = lane & 15;
    const unsigned vx0 = (lane >> 3) * 1024 + (((lane & 7) ^ r4) * 16);
    const unsigned vx1 = (lane >> 3) * 1024 + (((lane & 7) ^ (4 + r4)) * 16);
    const unsigned swT = (c16 ^ (r4 << 2)) * 16;
    const unsigned vwq = r4 * 8192 + swT, vv = r4 * 256 + swT, vwo = r4 * 1024 + swT;
    const unsigned vk = r4 * 256 + ((c16 ^ (4 * lw + r4)) * 16);
    const unsigned vlin = lane * 16;
    const unsigned xb = b * 65536 + lw * 16 * 1024, wqb = h * 256 + lw * 16 * 8192;
    const unsigned kvb = (unsigned)(b * 8 + kvh) * 131072u + lw * 1024;
    const unsigned wob = (unsigned)kvh * 512u * 1024u + g * 256 + lw * 4 * 1024;
    auto LA = [&](int j) {
      const unsigned sb = ldsb + (j % 5) * 3 * H;
      dma(sb + lw * 2048, vx0, sX, xb + j * 128);
      dma(sb + lw * 2048 + 1024, vx1, sX, xb + j * 128 + 8192);
#pragma unroll
      for (int p = 0; p < 4; ++p) dma(sb + H + lw * 4096 + p * 1024, vwq, sWq, wqb + (64 * j + 4 * p) * 8192);
    };
    auto LH = [&](__amdgpu_buffer_rsrc_t s, unsigned vo, int i, int ww, int hu) {
#pragma unroll
      for (int t = 0; t < 2; ++t) dma(ldsb + hu * H + lw * 1024 + t * 4096, vo, s, kvb + (128 * ww + 32 * i + 16 * t) * 256);
    };
    auto LK = [&](int i) {
#pragma unroll
      for (int ww = 0; ww < 4; ++ww) LH(sK, vk, i, ww, KH[i][ww]);
    };
    auto LV = [&](int i) {
#pragma unroll
      for (int ww = 0; ww < 4; ++ww) LH(sV, vv, i, ww, VH[i][ww]);
    };
    auto LWO = [&](int r, int q) {
      const unsigned hh = (g + r) & 3;
#pragma unroll
      for (int t = 0; t < 2; ++t) dma(ldsb + WH[r][q] * H + lw * 1024 + t * 4096, vwo, sWo, wob + (hh * 128 + 32 * q + 16 * t) * 1024);
    };
    LA(0); LA(1); LA(2);
    BAR_L(12); LA(3);
    BAR_L(12); LA(4);
    BAR_L(12); LA(5);
    BAR_L(12); LA(6);
    BAR_L(12); LA(7);
    BAR_L(12); LK(0);
    BAR_L(14); LK(1);
    BAR_L(16); LK(2);
    BAR_L(16); LK(3); LH(sV, vv, 0, 0, VH[0][0]); LH(sV, vv, 0, 1, VH[0][1]);
    BAR_L(20);
    BAR_L(12); LH(sV, vv, 0, 2, VH[0][2]); LH(sV, vv, 0, 3, VH[0][3]); LV(1);
    BAR_L(16); LV(2);
    BAR_L(63); LV(3);
    BAR_L(24);
    BAR_L(16); LWO(0, 0); LWO(0, 1); LWO(0, 2); LWO(0, 3);
    BAR_L(16); LWO(1, 0); LWO(1, 1); LWO(1, 2);
    BAR_L(14); LWO(1, 3); LWO(2, 0);
    BAR_L(10); LWO(2, 1); LWO(2, 2); LWO(2, 3);
    ST(1);
    {
      unsigned* f = g_flag1 + wid * 32 + (lane & 15);
      const bool mine = ((lane & 15) >> 2) == g;
      for (int it = 0; it < (1 << 16); ++it) {
        unsigned v = __hip_atomic_load(f, __ATOMIC_RELAXED, __HIP_MEMORY_SCOPE_AGENT);
        if (__builtin_amdgcn_ballot_w64(mine || v != 0) == ~0ull) break;
        __builtin_amdgcn_s_sleep(1);
      }
    }
    ST(2);
#pragma unroll
    for (int r = 1; r < 4; ++r) {
      const unsigned src = (unsigned)((b * 4 + ((g + r) & 3)) * 8 + kvh) * 16384u + lw * 4096;
#pragma unroll
      for (int p = 0; p < 4; ++p) dma_sc1(ldsb + OH[r] * H + lw * 4096 + p * 1024, vlin, sO, src + p * 1024);
    }
    BAR_L(0); LWO(3, 0); LWO(3, 1); LWO(3, 2); LWO(3, 3);
    ST(3);
    if (lw == 0 && lane < 16) __hip_atomic_store(g_flag1 + wid * 32 + lane, 0u, __ATOMIC_RELAXED, __HIP_MEMORY_SCOPE_AGENT);
    BAR_L(0);
    const int j = kvh * 4 + g;
    {
      unsigned* f = g_flag2 + (b * 32 + j) * 32 + lw * 8 + (lane & 7);
      for (int it = 0; it < (1 << 16); ++it) {
        unsigned v = __hip_atomic_load(f, __ATOMIC_RELAXED, __HIP_MEMORY_SCOPE_AGENT);
        if (__builtin_amdgcn_ballot_w64(v != 0) == ~0ull) break;
        __builtin_amdgcn_s_sleep(1);
      }
      ST(4);
      asm volatile("" ::: "memory");
      u32x4 v[8];
      const unsigned pb = (unsigned)((b * 32 + j) * 4 + lw) * 8192u;
#pragma unroll
      for (int k = 0; k < 8; ++k) v[k] = __builtin_amdgcn_raw_buffer_load_b128(sP, vlin, pb + k * 1024, 16);
      if (lane < 8) __hip_atomic_store(f, 0u, __ATOMIC_RELAXED, __HIP_MEMORY_SCOPE_AGENT);
      f32x4 a = __builtin_bit_cast(f32x4, v[0]);
#pragma unroll
      for (int k = 1; k < 8; ++k) a += __builtin_bit_cast(f32x4, v[k]);
      const int s = 32 * ((j >> 2) & 1) + (lane & 31), n = 128 * lw + 32 * (j >> 3) + 8 * (j & 3) + 4 * (lane >> 5);
      u32x2 o = {pk(a[0], a[1]), pk(a[2], a[3])};
      *(u32x2*)((char*)out + ((size_t)(b * 64 + s) * 512 + n) * 2) = o;
    }
    ST(5);
    return;
  }
  const int w = wave, hf = lane >> 5, l32 = lane & 31;
  const unsigned q4 = (lane & 15) >> 2, p4 = lane & 3, blk = (lane >> 4) & 1;
  const unsigned trc = 16 * ((4 * w + 2 * blk + (p4 >> 1)) ^ (q4 << 2)) + 8 * (p4 & 1);
  LAS char* trN = lds + 256 * (8 * hf + q4) + trc;
  LAS char* trP = lds + 256 * (4 * hf + q4) + trc;
  LAS char* lin = lds + lane * 16;
  constexpr float C = 0.08838834764831845f * 1.4426950408889634f;
  Frag f[4];
  f32x16 acc0 = {}, acc1 = {};
  {
    LAS char* xa[4];
#pragma unroll
    for (int ks = 0; ks < 4; ++ks) xa[ks] = lds + 128 * l32 + 16 * ((2 * ks + hf) ^ ((l32 >> 1) & 7));
#pragma unroll
    for (int t = 0; t < 35; ++t) {
      if (t < 32) {
        const int j = t >> 2, ks = t & 3;
        if (ks == 0) BAR();
        if (t == 0) ST(1);
        const unsigned sb = (j % 5) * 3 * H;
        f[t & 3].a = ldtr(trN + sb + H + ks * 4096, 1024);
        f[t & 3].b0 = ld128(xa[ks] + sb);
        f[t & 3].b1 = ld128(xa[ks] + sb + 4096);
      }
      if (t >= 3) {
        acc0 = mfma(f[(t - 3) & 3].a, f[(t - 3) & 3].b0, acc0);
        acc1 = mfma(f[(t - 3) & 3].a, f[(t - 3) & 3].b1, acc1);
      }
      SB();
    }
  }
  ST(2);
  {
#pragma unroll
    for (int nt = 0; nt < 2; ++nt) {
      const f32x16 a = nt ? acc1 : acc0;
#pragma unroll
      for (int ks = 0; ks < 2; ++ks) {
        unsigned r0 = pk(a[8 * ks + 0], a[8 * ks + 1]), r1 = pk(a[8 * ks + 2], a[8 * ks + 3]);
        unsigned r2 = pk(a[8 * ks + 4], a[8 * ks + 5]), r3 = pk(a[8 * ks + 6], a[8 * ks + 7]);
        u32x2 s0 = __builtin_amdgcn_permlane32_swap(r0, r2, false, false);
        u32x2 s1 = __builtin_amdgcn_permlane32_swap(r1, r3, false, false);
        u32x4 fq = {s0[0], s1[0], s0[1], s1[1]};
        *(LAS u32x4*)(lin + QH * H + ((2 * w + ks) * 2 + nt) * 1024) = fq;
      }
    }
  }
  BAR_C();
  ST(3);
  f32x16 S[4][2];
  float m[2] = {-INFINITY, -INFINITY};
  {
    bf16x8 qf[8][2], kf[4];
    LAS char* ka[8];
#pragma unroll
    for (int ks = 0; ks < 8; ++ks) ka[ks] = lds + 256 * l32 + 16 * ((2 * ks + hf) ^ (l32 & 15));
    unsigned ko[4];
#pragma unroll
    for (int i = 0; i < 4; ++i) ko[i] = ((pack4(KH[i]) >> (8 * w)) & 255) * H;
#pragma unroll
    for (int t = 0; t < 35; ++t) {
      if (t < 32) {
        const int i = t >> 3, ks = t & 7;
        if (ks == 0 && i) BAR();
        kf[t & 3] = ld128(ka[ks] + ko[i]);
        if (t < 8) { qf[t][0] = ld128(lin + QH * H + (t * 2) * 1024); qf[t][1] = ld128(lin + QH * H + (t * 2 + 1) * 1024); }
      }
      if (t >= 3) {
        const int u = t - 3, i = u >> 3, ks = u & 7;
        if (ks == 0) { S[i][0] = mfma(kf[u & 3], qf[ks][0], f32x16{}); S[i][1] = mfma(kf[u & 3], qf[ks][1], f32x16{}); }
        else { S[i][0] = mfma(kf[u & 3], qf[ks][0], S[i][0]); S[i][1] = mfma(kf[u & 3], qf[ks][1], S[i][1]); }
        if (i && ks >= 1 && ks <= 4) {
#pragma unroll
          for (int e = 0; e < 4; ++e) { m[0] = fmaxf(m[0], S[i - 1][0][4 * (ks - 1) + e]); m[1] = fmaxf(m[1], S[i - 1][1][4 * (ks - 1) + e]); }
        }
      }
      SB();
    }
  }
  ST(4);
  const unsigned areaW = (PH + w) * H;
  float mc[2];
  {
#pragma unroll
    for (int nt = 0; nt < 2; ++nt) {
#pragma unroll
      for (int e = 0; e < 16; ++e) m[nt] = fmaxf(m[nt], S[3][nt][e]);
    }
    u32x2 sw = __builtin_amdgcn_permlane32_swap(__builtin_bit_cast(unsigned, m[0]), __builtin_bit_cast(unsigned, m[1]), false, false);
    *(LAS float*)(lds + areaW + lane * 4) = fmaxf(__uint_as_float(sw.x), __uint_as_float(sw.y));
    BAR_C();
#pragma unroll
    for (int nt = 0; nt < 2; ++nt) {
      float t = -INFINITY;
#pragma unroll
      for (int ww = 0; ww < 4; ++ww) t = fmaxf(t, *(LAS float*)(lds + (PH + ww) * H + (32 * nt + l32) * 4));
      mc[nt] = t * C;
    }
  }
  ST(5);
  float ls[2] = {0.f, 0.f};
  acc0 = f32x16{}; acc1 = f32x16{};
  {
    unsigned pp[4];
#pragma unroll
    for (int t = -8; t < 35; ++t) {
      if (t >= 0 && t < 32) {
        const int i = t >> 3, ww = (t >> 1) & 3, ks = t & 1;
        if ((t & 7) == 0) BAR_C();
        f[t & 3].a = ldtr(trP + VH[i][ww] * H + ks * 4096, 2048);
        const unsigned po = (PH + ww) * H + (1 - (i & 1)) * 4096 + ks * 2048;
        f[t & 3].b0 = ld128(lin + po);
        f[t & 3].b1 = ld128(lin + po + 1024);
      }
      if (t >= 3) {
        acc0 = mfma(f[(t - 3) & 3].a, f[(t - 3) & 3].b0, acc0);
        acc1 = mfma(f[(t - 3) & 3].a, f[(t - 3) & 3].b1, acc1);
      }
      if (t < 24) {
        const int i = (t + 8) >> 3, u = (t + 8) & 7, nt = u >> 2, e0 = (u & 3) * 4;
        float p[4];
#pragma unroll
        for (int e = 0; e < 4; ++e) { p[e] = __builtin_amdgcn_exp2f(__builtin_fmaf(S[i][nt][e0 + e], C, -mc[nt])); ls[nt] += p[e]; }
        pp[(u & 1) * 2] = pk(p[0], p[1]);
        pp[(u & 1) * 2 + 1] = pk(p[2], p[3]);
        if (u & 1) {
          u32x4 fp = {pp[0], pp[1], pp[2], pp[3]};
          *(LAS u32x4*)(lin + areaW + (1 - (i & 1)) * 4096 + (((u >> 1) & 1) * 2 + nt) * 1024) = fp;
        }
        if (t == 23) {
          u32x2 sw = __builtin_amdgcn_permlane32_swap(__builtin_bit_cast(unsigned, ls[0]), __builtin_bit_cast(unsigned, ls[1]), false, false);
          *(LAS float*)(lds + SUMH * H + w * 256 + lane * 4) = __uint_as_float(sw.x) + __uint_as_float(sw.y);
        }
      }
      if (t == 26) {
#pragma unroll
        for (int nt = 0; nt < 2; ++nt) {
          float l = 0.f;
#pragma unroll
          for (int ww = 0; ww < 4; ++ww) l += *(LAS float*)(lds + SUMH * H + ww * 256 + (32 * nt + l32) * 4);
          ls[nt] = 1.0f / l;
        }
      }
      SB();
    }
  }
  ST(6);
  {
    const unsigned ob = (unsigned)wid * 16384u + w * 4096;
#pragma unroll
    for (int nt = 0; nt < 2; ++nt) {
      const float inv = ls[nt];
      const f32x16 a = nt ? acc1 : acc0;
#pragma unroll
      for (int ks = 0; ks < 2; ++ks) {
        u32x4 fo = {pk(a[8 * ks] * inv, a[8 * ks + 1] * inv), pk(a[8 * ks + 2] * inv, a[8 * ks + 3] * inv),
                    pk(a[8 * ks + 4] * inv, a[8 * ks + 5] * inv), pk(a[8 * ks + 6] * inv, a[8 * ks + 7] * inv)};
        __builtin_amdgcn_raw_buffer_store_b128(fo, sO, lane * 16, ob + (ks * 2 + nt) * 1024, 16);
        *(LAS u32x4*)(lin + OH[0] * H + w * 4096 + (ks * 2 + nt) * 1024) = fo;
      }
    }
  }
  acc0 = f32x16{}; acc1 = f32x16{};
#pragma unroll
  for (int t = 0; t < 38; ++t) {
    const int tr = t < 11 ? t : t - 3;
    const bool rd = t < 8 || (t >= 11 && t < 35);
    if (t == 0) { BAR_C(); ST(7); }
    if (t == 11) {
      asm volatile("s_waitcnt vmcnt(0)" ::: "memory");
      if (lane < 4 && lane != g) __hip_atomic_store(g_flag1 + ((b * 4 + lane) * 8 + kvh) * 32 + g * 4 + w, 1u, __ATOMIC_RELAXED, __HIP_MEMORY_SCOPE_AGENT);
      ST(11);
      BAR();
      ST(8);
    }
    if (t == 27) BAR();
    if (rd) {
      const int r = tr >> 3, k8 = tr & 7;
      f[t & 3].a = ldtr(trP + WH[r][k8 >> 1] * H + (k8 & 1) * 4096, 2048);
      f[t & 3].b0 = ld128(lin + OH[r] * H + k8 * 2048);
      f[t & 3].b1 = ld128(lin + OH[r] * H + k8 * 2048 + 1024);
    }
    if ((t >= 3 && t < 11) || t >= 14) {
      acc0 = mfma(f[(t - 3) & 3].a, f[(t - 3) & 3].b0, acc0);
      acc1 = mfma(f[(t - 3) & 3].a, f[(t - 3) & 3].b1, acc1);
    }
    SB();
  }
  ST(9);
  {
#pragma unroll
    for (int nt = 0; nt < 2; ++nt) {
      const f32x16 a = nt ? acc1 : acc0;
#pragma unroll
      for (int qd = 0; qd < 4; ++qd) {
        f32x4 fo = {a[4 * qd], a[4 * qd + 1], a[4 * qd + 2], a[4 * qd + 3]};
        const unsigned j = w * 8 + nt * 4 + qd;
        __builtin_amdgcn_raw_buffer_store_b128(__builtin_bit_cast(u32x4, fo), sP, lane * 16, (unsigned)(((b * 32 + j) * 4 + g) * 8 + kvh) * 1024u, 16);
      }
    }
    asm volatile("s_waitcnt vmcnt(0)" ::: "memory");
    if (lane < 8) __hip_atomic_store(g_flag2 + (b * 32 + w * 8 + lane) * 32 + g * 8 + kvh, 1u, __ATOMIC_RELAXED, __HIP_MEMORY_SCOPE_AGENT);
  }
  ST(10);
}

extern "C" void kernel_launch(void* const* d_in, const int* in_sizes, int n_in, void* d_out, int out_size, void* d_ws, size_t ws_size,
                              hipStream_t stream) {
  (void)hipFuncSetAttribute((const void*)gqa_fused, hipFuncAttributeMaxDynamicSharedMemorySize, 163840);
  gqa_fused<<<256, 512, 163840, stream>>>(d_in[0], d_in[1], d_in[2], d_in[3], d_in[4], d_out, (char*)d_ws);
}
```

```cpp
#define PADK 1
#define PADROLE 0
#include <hip/hip_runtime.h>
#include <hip/hip_bf16.h>

typedef __attribute__((ext_vector_type(8))) __bf16 bf16x8;
typedef __attribute__((ext_vector_type(2))) __bf16 bf16x2;
typedef __attribute__((ext_vector_type(4))) short s16x4;
typedef __attribute__((ext_vector_type(8))) short s16x8;
typedef __attribute__((ext_vector_type(16))) float f32x16;
typedef __attribute__((ext_vector_type(4))) float f32x4;
typedef __attribute__((ext_vector_type(4))) unsigned u32x4;
typedef __attribute__((ext_vector_type(2))) unsigned u32x2;
#define LAS __attribute__((address_space(3)))
#define DEV __device__ __forceinline__

__device__ unsigned g_flag1[256 * 32];
__device__ unsigned g_flag2[256 * 32];

#define ST(i) do { if ((i) == 0) tstart = __builtin_amdgcn_s_memrealtime(); else if ((i) == PADK && (wave >= 4) == PADROLE) tk = __builtin_amdgcn_s_memrealtime(); } while (0)
#define PADEND() do { unsigned long long lim = tstart + 4000 + (tk ? tk - tstart : 0); for (int it = 0; it < 100000; ++it) { if (__builtin_amdgcn_s_memrealtime() >= lim) break; __builtin_amdgcn_s_sleep(2); } } while (0)

constexpr unsigned H = 8192;
#define BAR() asm volatile("s_barrier" ::: "memory")
#define BAR_C() asm volatile("s_waitcnt lgkmcnt(0)\n\ts_barrier" ::: "memory")
#define BAR_L(n) asm volatile("s_waitcnt vmcnt(%0)\n\ts_barrier" ::"i"(n) : "memory")
#define SB() __builtin_amdgcn_sched_barrier(0)

DEV void dma(unsigned m0v, unsigned voff, __amdgpu_buffer_rsrc_t srd, unsigned soff) {
  asm volatile("s_mov_b32 m0, %0\n\ts_nop 0\n\tbuffer_load_dwordx4 %1, %2, %3 offen lds" ::"s"(m0v), "v"(voff), "s"(srd), "s"(soff) : "memory");
}
DEV void dma_sc1(unsigned m0v, unsigned voff, __amdgpu_buffer_rsrc_t srd, unsigned soff) {
  asm volatile("s_mov_b32 m0, %0\n\ts_nop 0\n\tbuffer_load_dwordx4 %1, %2, %3 offen sc1 lds" ::"s"(m0v), "v"(voff), "s"(srd), "s"(soff) : "memory");
}
DEV bf16x8 ld128(LAS char* p) { return *(LAS bf16x8*)p; }
DEV bf16x8 ldtr(LAS char* p, unsigned tstep) {
  s16x4 a = __builtin_amdgcn_ds_read_tr16_b64_v4i16((LAS s16x4*)p);
  s16x4 b = __builtin_amdgcn_ds_read_tr16_b64_v4i16((LAS s16x4*)(p + tstep));
  s16x8 r = __builtin_shufflevector(a, b, 0, 1, 2, 3, 4, 5, 6, 7);
  return __builtin_bit_cast(bf16x8, r);
}
DEV unsigned pk(float a, float b) { bf16x2 r = {(__bf16)a, (__bf16)b}; return __builtin_bit_cast(unsigned, r); }
DEV f32x16 mfma(bf16x8 a, bf16x8 b, f32x16 c) { return __builtin_amdgcn_mfma_f32_32x32x16_bf16(a, b, c, 0, 0, 0); }

constexpr int KH[4][4] = {{15, 16, 17, 18}, {19, 9, 10, 11}, {12, 13, 14, 0}, {3, 4, 5, 6}};
constexpr int VH[4][4] = {{7, 8, 1, 2}, {15, 16, 17, 18}, {19, 9, 10, 11}, {12, 13, 14, 0}};
constexpr int WH[4][4] = {{7, 8, 1, 2}, {15, 16, 17, 19}, {11, 14, 0, 18}, {7, 8, 1, 2}};
constexpr int OH[4] = {9, 12, 3, 5};
constexpr int QH = 1, SUMH = 18, PH = 3;
constexpr unsigned pack4(const int* a) { return a[0] | (a[1] << 8) | (a[2] << 16) | (a[3] << 24); }

struct Frag { bf16x8 a, b0, b1; };

__global__ __launch_bounds__(512, 2) void gqa_fused(const void* __restrict__ x, const void* __restrict__ kc, const void* __restrict__ vc,
                                                    const void* __restrict__ wq, const void* __restrict__ wo, void* __restrict__ out, char* __restrict__ ws) {
  extern __shared__ __attribute__((aligned(16))) char smem[];
  LAS char* lds = (LAS char*)smem;
  const unsigned ldsb = (unsigned)(size_t)lds;
  const int wid = blockIdx.x, kvh = wid & 7, g = (wid >> 3) & 3, b = wid >> 5, h = kvh * 4 + g;
  const int wave = __builtin_amdgcn_readfirstlane(threadIdx.x >> 6), lane = threadIdx.x & 63;
  const __amdgpu_buffer_rsrc_t sO = __builtin_amdgcn_make_buffer_rsrc(ws, 0, 4u << 20, 0x00020000);
  const __amdgpu_buffer_rsrc_t sP = __builtin_amdgcn_make_buffer_rsrc(ws + (4u << 20), 0, 8u << 20, 0x00020000);
  unsigned long long tstart = 0, tk = 0;
  ST(0);
  if (wave >= 4) {
    const int lw = wave - 4;
    const __amdgpu_buffer_rsrc_t sX = __builtin_amdgcn_make_buffer_rsrc((void*)x, 0, 8u * 64 * 512 * 2, 0x00020000);
    const __amdgpu_buffer_rsrc_t sK = __builtin_amdgcn_make_buffer_rsrc((void*)kc, 0, 8u << 20, 0x00020000);
    const __amdgpu_buffer_rsrc_t sV = __builtin_amdgcn_make_buffer_rsrc((void*)vc, 0, 8u << 20, 0x00020000);
    const __amdgpu_buffer_rsrc_t sWq = __builtin_amdgcn_make_buffer_rsrc((void*)wq, 0, 4u << 20, 0x00020000);
    const __amdgpu_buffer_rsrc_t sWo = __builtin_amdgcn_make_buffer_rsrc((void*)wo, 0, 4u << 20, 0x00020000);
    const unsigned r4 = lane >> 4, c16 = lane & 15;
    const unsigned vx0 = (lane >> 3) * 1024 + (((lane & 7) ^ r4) * 16);
    const unsigned vx1 = (lane >> 3) * 1024 + (((lane & 7) ^ (4 + r4)) * 16);
    const unsigned swT = (c16 ^ (r4 << 2)) * 16;
    const unsigned vwq = r4 * 8192 + swT, vv = r4 * 256 + swT, vwo = r4 * 1024 + swT;
    const unsigned vk = r4 * 256 + ((c16 ^ (4 * lw + r4)) * 16);
    const unsigned vlin = lane * 16;
    const unsigned xb = b * 65536 + lw * 16 * 1024, wqb = h * 256 + lw * 16 * 8192;
    const unsigned kvb = (unsigned)(b * 8 + kvh) * 131072u + lw * 1024;
    const unsigned wob = (unsigned)kvh * 512u * 1024u + g * 256 + lw * 4 * 1024;
    auto LA = [&](int j) {
      const unsigned sb = ldsb + (j % 5) * 3 * H;
      dma(sb + lw * 2048, vx0, sX, xb + j * 128);
      dma(sb + lw * 2048 + 1024, vx1, sX, xb + j * 128 + 8192);
#pragma unroll
      for (int p = 0; p < 4; ++p) dma(sb + H + lw * 4096 + p * 1024, vwq, sWq, wqb + (64 * j + 4 * p) * 8192);
    };
    auto LH = [&](__amdgpu_buffer_rsrc_t s, unsigned vo, int i, int ww, int hu) {
#pragma unroll
      for (int t = 0; t < 2; ++t) dma(ldsb + hu * H + lw * 1024 + t * 4096, vo, s, kvb + (128 * ww + 32 * i + 16 * t) * 256);
    };
    auto LK = [&](int i) {
#pragma unroll
      for (int ww = 0; ww < 4; ++ww) LH(sK, vk, i, ww, KH[i][ww]);
    };
    auto LV = [&](int i) {
#pragma unroll
      for (int ww = 0; ww < 4; ++ww) LH(sV, vv, i, ww, VH[i][ww]);
    };
    auto LWO = [&](int r, int q) {
      const unsigned hh = (g + r) & 3;
#pragma unroll
      for (int t = 0; t < 2; ++t) dma(ldsb + WH[r][q] * H + lw * 1024 + t * 4096, vwo, sWo, wob + (hh * 128 + 32 * q + 16 * t) * 1024);
    };
    LA(0); LA(1); LA(2);
    BAR_L(12); LA(3);
    BAR_L(12); LA(4);
    BAR_L(12); LA(5);
    BAR_L(12); LA(6);
    BAR_L(12); LA(7);
    BAR_L(12); LK(0);
    BAR_L(14); LK(1);
    BAR_L(16); LK(2);
    BAR_L(16); LK(3); LH(sV, vv, 0, 0, VH[0][0]); LH(sV, vv, 0, 1, VH[0][1]);
    BAR_L(20);
    BAR_L(12); LH(sV, vv, 0, 2, VH[0][2]); LH(sV, vv, 0, 3, VH[0][3]); LV(1);
    BAR_L(16); LV(2);
    BAR_L(63); LV(3);
    BAR_L(24);
    BAR_L(16); LWO(0, 0); LWO(0, 1); LWO(0, 2); LWO(0, 3);
    BAR_L(16); LWO(1, 0); LWO(1, 1); LWO(1, 2);
    BAR_L(14); LWO(1, 3); LWO(2, 0);
    BAR_L(10); LWO(2, 1); LWO(2, 2); LWO(2, 3);
    ST(1);
    {
      unsigned* f = g_flag1 + wid * 32 + (lane & 15);
      const bool mine = ((lane & 15) >> 2) == g;
      for (int it = 0; it < (1 << 16); ++it) {
        unsigned v = __hip_atomic_load(f, __ATOMIC_RELAXED, __HIP_MEMORY_SCOPE_AGENT);
        if (__builtin_amdgcn_ballot_w64(mine || v != 0) == ~0ull) break;
        __builtin_amdgcn_s_sleep(1);
      }
    }
    ST(2);
#pragma unroll
    for (int r = 1; r < 4; ++r) {
      const unsigned src = (unsigned)((b * 4 + ((g + r) & 3)) * 8 + kvh) * 16384u + lw * 4096;
#pragma unroll
      for (int p = 0; p < 4; ++p) dma_sc1(ldsb + OH[r] * H + lw * 4096 + p * 1024, vlin, sO, src + p * 1024);
    }
    BAR_L(0); LWO(3, 0); LWO(3, 1); LWO(3, 2); LWO(3, 3);
    ST(3);
    if (lw == 0 && lane < 16) __hip_atomic_store(g_flag1 + wid * 32 + lane, 0u, __ATOMIC_RELAXED, __HIP_MEMORY_SCOPE_AGENT);
    BAR_L(0);
    const int j = kvh * 4 + g;
    {
      unsigned* f = g_flag2 + (b * 32 + j) * 32 + lw * 8 + (lane & 7);
      for (int it = 0; it < (1 << 16); ++it) {
        unsigned v = __hip_atomic_load(f, __ATOMIC_RELAXED, __HIP_MEMORY_SCOPE_AGENT);
        if (__builtin_amdgcn_ballot_w64(v != 0) == ~0ull) break;
        __builtin_amdgcn_s_sleep(1);
      }
      ST(4);
      asm volatile("" ::: "memory");
      u32x4 v[8];
      const unsigned pb = (unsigned)((b * 32 + j) * 4 + lw) * 8192u;
#pragma unroll
      for (int k = 0; k < 8; ++k) v[k] = __builtin_amdgcn_raw_buffer_load_b128(sP, vlin, pb + k * 1024, 16);
      if (lane < 8) __hip_atomic_store(f, 0u, __ATOMIC_RELAXED, __HIP_MEMORY_SCOPE_AGENT);
      f32x4 a = __builtin_bit_cast(f32x4, v[0]);
#pragma unroll
      for (int k = 1; k < 8; ++k) a += __builtin_bit_cast(f32x4, v[k]);
      const int s = 32 * ((j >> 2) & 1) + (lane & 31), n = 128 * lw + 32 * (j >> 3) + 8 * (j & 3) + 4 * (lane >> 5);
      u32x2 o = {pk(a[0], a[1]), pk(a[2], a[3])};
      *(u32x2*)((char*)out + ((size_t)(b * 64 + s) * 512 + n) * 2) = o;
    }
    ST(5);
    PADEND();
    return;
  }
  const int w = wave, hf = lane >> 5, l32 = lane & 31;
  const unsigned q4 = (lane & 15) >> 2, p4 = lane & 3, blk = (lane >> 4) & 1;
  const unsigned trc = 16 * ((4 * w + 2 * blk + (p4 >> 1)) ^ (q4 << 2)) + 8 * (p4 & 1);
  LAS char* trN = lds + 256 * (8 * hf + q4) + trc;
  LAS char* trP = lds + 256 * (4 * hf + q4) + trc;
  LAS char* lin = lds + lane * 16;
  constexpr float C = 0.08838834764831845f * 1.4426950408889634f;
  Frag f[4];
  f32x16 acc0 = {}, acc1 = {};
  {
    LAS char* xa[4];
#pragma unroll
    for (int ks = 0; ks < 4; ++ks) xa[ks] = lds + 128 * l32 + 16 * ((2 * ks + hf) ^ ((l32 >> 1) & 7));
#pragma unroll
    for (int t = 0; t < 35; ++t) {
      if (t < 32) {
        const int j = t >> 2, ks = t & 3;
        if (ks == 0) BAR();
        if (t == 0) ST(1);
        const unsigned sb = (j % 5) * 3 * H;
        f[t & 3].a = ldtr(trN + sb + H + ks * 4096, 1024);
        f[t & 3].b0 = ld128(xa[ks] + sb);
        f[t & 3].b1 = ld128(xa[ks] + sb + 4096);
      }
      if (t >= 3) {
        acc0 = mfma(f[(t - 3) & 3].a, f[(t - 3) & 3].b0, acc0);
        acc1 = mfma(f[(t - 3) & 3].a, f[(t - 3) & 3].b1, acc1);
      }
      SB();
    }
  }
  ST(2);
  {
#pragma unroll
    for (int nt = 0; nt < 2; ++nt) {
      const f32x16 a = nt ? acc1 : acc0;
#pragma unroll
      for (int ks = 0; ks < 2; ++ks) {
        unsigned r0 = pk(a[8 * ks + 0], a[8 * ks + 1]), r1 = pk(a[8 * ks + 2], a[8 * ks + 3]);
        unsigned r2 = pk(a[8 * ks + 4], a[8 * ks + 5]), r3 = pk(a[8 * ks + 6], a[8 * ks + 7]);
        u32x2 s0 = __builtin_amdgcn_permlane32_swap(r0, r2, false, false);
        u32x2 s1 = __builtin_amdgcn_permlane32_swap(r1, r3, false, false);
        u32x4 fq = {s0[0], s1[0], s0[1], s1[1]};
        *(LAS u32x4*)(lin + QH * H + ((2 * w + ks) * 2 + nt) * 1024) = fq;
      }
    }
  }
  BAR_C();
  ST(3);
  f32x16 S[4][2];
  float m[2] = {-INFINITY, -INFINITY};
  {
    bf16x8 qf[8][2], kf[4];
    LAS char* ka[8];
#pragma unroll
    for (int ks = 0; ks < 8; ++ks) ka[ks] = lds + 256 * l32 + 16 * ((2 * ks + hf) ^ (l32 & 15));
    unsigned ko[4];
#pragma unroll
    for (int i = 0; i < 4; ++i) ko[i] = ((pack4(KH[i]) >> (8 * w)) & 255) * H;
#pragma unroll
    for (int t = 0; t < 35; ++t) {
      if (t < 32) {
        const int i = t >> 3, ks = t & 7;
        if (ks == 0 && i) BAR();
        kf[t & 3] = ld128(ka[ks] + ko[i]);
        if (t < 8) { qf[t][0] = ld128(lin + QH * H + (t * 2) * 1024); qf[t][1] = ld128(lin + QH * H + (t * 2 + 1) * 1024); }
      }
      if (t >= 3) {
        const int u = t - 3, i = u >> 3, ks = u & 7;
        if (ks == 0) { S[i][0] = mfma(kf[u & 3], qf[ks][0], f32x16{}); S[i][1] = mfma(kf[u & 3], qf[ks][1], f32x16{}); }
        else { S[i][0] = mfma(kf[u & 3], qf[ks][0], S[i][0]); S[i][1] = mfma(kf[u & 3], qf[ks][1], S[i][1]); }
        if (i && ks >= 1 && ks <= 4) {
#pragma unroll
          for (int e = 0; e < 4; ++e) { m[0] = fmaxf(m[0], S[i - 1][0][4 * (ks - 1) + e]); m[1] = fmaxf(m[1], S[i - 1][1][4 * (ks - 1) + e]); }
        }
      }
      SB();
    }
  }
  ST(4);
  const unsigned areaW = (PH + w) * H;
  float mc[2];
  {
#pragma unroll
    for (int nt = 0; nt < 2; ++nt) {
#pragma unroll
      for (int e = 0; e < 16; ++e) m[nt] = fmaxf(m[nt], S[3][nt][e]);
    }
    u32x2 sw = __builtin_amdgcn_permlane32_swap(__builtin_bit_cast(unsigned, m[0]), __builtin_bit_cast(unsigned, m[1]), false, false);
    *(LAS float*)(lds + areaW + lane * 4) = fmaxf(__uint_as_float(sw.x), __uint_as_float(sw.y));
    BAR_C();
#pragma unroll
    for (int nt = 0; nt < 2; ++nt) {
      float t = -INFINITY;
#pragma unroll
      for (int ww = 0; ww < 4; ++ww) t = fmaxf(t, *(LAS float*)(lds + (PH + ww) * H + (32 * nt + l32) * 4));
      mc[nt] = t * C;
    }
  }
  ST(5);
  float ls[2] = {0.f, 0.f};
  acc0 = f32x16{}; acc1 = f32x16{};
  {
    unsigned pp[4];
#pragma unroll
    for (int t = -8; t < 35; ++t) {
      if (t >= 0 && t < 32) {
        const int i = t >> 3, ww = (t >> 1) & 3, ks = t & 1;
        if ((t & 7) == 0) BAR_C();
        f[t & 3].a = ldtr(trP + VH[i][ww] * H + ks * 4096, 2048);
        const unsigned po = (PH + ww) * H + (1 - (i & 1)) * 4096 + ks * 2048;
        f[t & 3].b0 = ld128(lin + po);
        f[t & 3].b1 = ld128(lin + po + 1024);
      }
      if (t >= 3) {
        acc0 = mfma(f[(t - 3) & 3].a, f[(t - 3) & 3].b0, acc0);
        acc1 = mfma(f[(t - 3) & 3].a, f[(t - 3) & 3].b1, acc1);
      }
      if (t < 24) {
        const int i = (t + 8) >> 3, u = (t + 8) & 7, nt = u >> 2, e0 = (u & 3) * 4;
        float p[4];
#pragma unroll
        for (int e = 0; e < 4; ++e) { p[e] = __builtin_amdgcn_exp2f(__builtin_fmaf(S[i][nt][e0 + e], C, -mc[nt])); ls[nt] += p[e]; }
        pp[(u & 1) * 2] = pk(p[0], p[1]);
        pp[(u & 1) * 2 + 1] = pk(p[2], p[3]);
        if (u & 1) {
          u32x4 fp = {pp[0], pp[1], pp[2], pp[3]};
          *(LAS u32x4*)(lin + areaW + (1 - (i & 1)) * 4096 + (((u >> 1) & 1) * 2 + nt) * 1024) = fp;
        }
        if (t == 23) {
          u32x2 sw = __builtin_amdgcn_permlane32_swap(__builtin_bit_cast(unsigned, ls[0]), __builtin_bit_cast(unsigned, ls[1]), false, false);
          *(LAS float*)(lds + SUMH * H + w * 256 + lane * 4) = __uint_as_float(sw.x) + __uint_as_float(sw.y);
        }
      }
      if (t == 26) {
#pragma unroll
        for (int nt = 0; nt < 2; ++nt) {
          float l = 0.f;
#pragma unroll
          for (int ww = 0; ww < 4; ++ww) l += *(LAS float*)(lds + SUMH * H + ww * 256 + (32 * nt + l32) * 4);
          ls[nt] = 1.0f / l;
        }
      }
      SB();
    }
  }
  ST(6);
  {
    const unsigned ob = (unsigned)wid * 16384u + w * 4096;
#pragma unroll
    for (int nt = 0; nt < 2; ++nt) {
      const float inv = ls[nt];
      const f32x16 a = nt ? acc1 : acc0;
#pragma unroll
      for (int ks = 0; ks < 2; ++ks) {
        u32x4 fo = {pk(a[8 * ks] * inv, a[8 * ks + 1] * inv), pk(a[8 * ks + 2] * inv, a[8 * ks + 3] * inv),
                    pk(a[8 * ks + 4] * inv, a[8 * ks + 5] * inv), pk(a[8 * ks + 6] * inv, a[8 * ks + 7] * inv)};
        __builtin_amdgcn_raw_buffer_store_b128(fo, sO, lane * 16, ob + (ks * 2 + nt) * 1024, 16);
        *(LAS u32x4*)(lin + OH[0] * H + w * 4096 + (ks * 2 + nt) * 1024) = fo;
      }
    }
  }
  acc0 = f32x16{}; acc1 = f32x16{};
#pragma unroll
  for (int t = 0; t < 38; ++t) {
    const int tr = t < 11 ? t : t - 3;
    const bool rd = t < 8 || (t >= 11 && t < 35);
    if (t == 0) { BAR_C(); ST(7); }
    if (t == 11) {
      asm volatile("s_waitcnt vmcnt(0)" ::: "memory");
      if (lane < 4 && lane != g) __hip_atomic_store(g_flag1 + ((b * 4 + lane) * 8 + kvh) * 32 + g * 4 + w, 1u, __ATOMIC_RELAXED, __HIP_MEMORY_SCOPE_AGENT);
      ST(11);
      BAR();
      ST(8);
    }
    if (t == 27) BAR();
    if (rd) {
      const int r = tr >> 3, k8 = tr & 7;
      f[t & 3].a = ldtr(trP + WH[r][k8 >> 1] * H + (k8 & 1) * 4096, 2048);
      f[t & 3].b0 = ld128(lin + OH[r] * H + k8 * 2048);
      f[t & 3].b1 = ld128(lin + OH[r] * H + k8 * 2048 + 1024);
    }
    if ((t >= 3 && t < 11) || t >= 14) {
      acc0 = mfma(f[(t - 3) & 3].a, f[(t - 3) & 3].b0, acc0);
      acc1 = mfma(f[(t - 3) & 3].a, f[(t - 3) & 3].b1, acc1);
    }
    SB();
  }
  ST(9);
  {
#pragma unroll
    for (int nt = 0; nt < 2; ++nt) {
      const f32x16 a = nt ? acc1 : acc0;
#pragma unroll
      for (int qd = 0; qd < 4; ++qd) {
        f32x4 fo = {a[4 * qd], a[4 * qd + 1], a[4 * qd + 2], a[4 * qd + 3]};
        const unsigned j = w * 8 + nt * 4 + qd;
        __builtin_amdgcn_raw_buffer_store_b128(__builtin_bit_cast(u32x4, fo), sP, lane * 16, (unsigned)(((b * 32 + j) * 4 + g) * 8 + kvh) * 1024u, 16);
      }
    }
    asm volatile("s_waitcnt vmcnt(0)" ::: "memory");
    if (lane < 8) __hip_atomic_store(g_flag2 + (b * 32 + w * 8 + lane) * 32 + g * 8 + kvh, 1u, __ATOMIC_RELAXED, __HIP_MEMORY_SCOPE_AGENT);
  }
  ST(10);
  PADEND();
}

extern "C" void kernel_launch(void* const* d_in, const int* in_sizes, int n_in, void* d_out, int out_size, void* d_ws, size_t ws_size,
                              hipStream_t stream) {
  (void)hipFuncSetAttribute((const void*)gqa_fused, hipFuncAttributeMaxDynamicSharedMemorySize, 163840);
  gqa_fused<<<256, 512, 163840, stream>>>(d_in[0], d_in[1], d_in[2], d_in[3], d_in[4], d_out, (char*)d_ws);
}
```

```cpp
#include <hip/hip_runtime.h>
#include <hip/hip_bf16.h>

typedef __attribute__((ext_vector_type(8))) __bf16 bf16x8;
typedef __attribute__((ext_vector_type(2))) __bf16 bf16x2;
typedef __attribute__((ext_vector_type(4))) short s16x4;
typedef __attribute__((ext_vector_type(8))) short s16x8;
typedef __attribute__((ext_vector_type(16))) float f32x16;
typedef __attribute__((ext_vector_type(4))) float f32x4;
typedef __attribute__((ext_vector_type(4))) unsigned u32x4;
typedef __attribute__((ext_vector_type(2))) unsigned u32x2;
typedef unsigned long long u64;
#ifdef __HIP_DEVICE_COMPILE__
#pragma clang attribute push(__attribute__((target("no-packed-fp32-ops"))), apply_to = function)
#endif
#define LAS __attribute__((address_space(3)))
#define DEV __device__ __forceinline__

#define STN()
#define KATTR

#define CAPA 8
#define INITN 9
#define E3 10
#define TF 8
#define M7 36
#define FILL 0, 2, 4, 6, 8, 10, 16, 16, 16

constexpr unsigned H = 8192;
#define BAR() do { STN(); asm volatile("s_barrier" ::: "memory"); STN(); } while (0)
#define BAR_C() do { asm volatile("s_waitcnt lgkmcnt(0)" ::: "memory"); BAR(); } while (0)
#define SB() __builtin_amdgcn_sched_barrier(0)
#define PIN(p) asm volatile("" : "+v"(p))
#define RLX __ATOMIC_RELAXED, __HIP_MEMORY_SCOPE_AGENT

DEV unsigned f2u(float f) { return __builtin_bit_cast(unsigned, f); }
DEV float u2f(unsigned u) { return __builtin_bit_cast(float, u); }
DEV void dma(unsigned m0v, unsigned voff, __amdgpu_buffer_rsrc_t srd, unsigned soff) {
  asm volatile("s_mov_b32 m0, %0\n\ts_nop 0\n\tbuffer_load_dwordx4 %1, %2, %3 offen lds" ::"s"(m0v), "v"(voff), "s"(srd), "s"(soff) : "memory");
}
DEV bf16x8 ld128(LAS char* p) { return *(LAS bf16x8*)p; }
DEV bf16x8 ldtr(LAS char* p, unsigned tstep) {
  s16x4 a = __builtin_amdgcn_ds_read_tr16_b64_v4i16((LAS s16x4*)p);
  s16x4 b = __builtin_amdgcn_ds_read_tr16_b64_v4i16((LAS s16x4*)(p + tstep));
  s16x8 r = __builtin_shufflevector(a, b, 0, 1, 2, 3, 4, 5, 6, 7);
  return __builtin_bit_cast(bf16x8, r);
}
DEV unsigned pk(float a, float b) { bf16x2 r = {(__bf16)a, (__bf16)b}; return __builtin_bit_cast(unsigned, r); }
DEV f32x16 mfma(bf16x8 a, bf16x8 b, f32x16 c) { return __builtin_amdgcn_mfma_f32_32x32x16_bf16(a, b, c, 0, 0, 0); }
DEV float fmax2(float a, float b) { return __builtin_fmaxf(a, b); }
DEV float max4(f32x4 v) { return fmax2(fmax2(v.x, v.y), fmax2(v.z, v.w)); }
DEV float ex2(float a) { return __builtin_amdgcn_exp2f(a); }
DEV u32x4 pack8(const f32x16& a, int ks) {
  unsigned r0 = pk(a[8 * ks + 0], a[8 * ks + 1]), r1 = pk(a[8 * ks + 2], a[8 * ks + 3]);
  unsigned r2 = pk(a[8 * ks + 4], a[8 * ks + 5]), r3 = pk(a[8 * ks + 6], a[8 * ks + 7]);
  u32x2 s0 = __builtin_amdgcn_permlane32_swap(r0, r2, false, false);
  u32x2 s1 = __builtin_amdgcn_permlane32_swap(r1, r3, false, false);
  return u32x4{s0.x, s1.x, s0.y, s1.y};
}
DEV unsigned rot(unsigned v, int r) { return __builtin_amdgcn_alignbit(v, v, r); }
DEV u32x2 seal(u32x4 a, u32x4 b, unsigned t0, unsigned t1) {
  const unsigned s = (a.x + a.y + a.z) + (a.w + b.x + b.y) + (b.z + b.w + t0);
  const unsigned x = (a.x ^ rot(a.y, 3) ^ rot(a.z, 7)) ^ (rot(a.w, 11) ^ rot(b.x, 13) ^ rot(b.y, 17)) ^ (rot(b.z, 23) ^ rot(b.w, 29) ^ t1);
  return u32x2{s, x};
}

constexpr int SLOT[72] = {0, 1, 2, 3, 4, 5, 6, 7, 8, 9, 10, 11, 12, 13, 14, 15, 16, 17, 18, 19, 0, 1, 2, 3,
                          4, 5, 6, 7, 8, 9, 10, 11, 12, 13, 14, 15, 0, 1, 2, 3,
                          4, 5, 6, 7, 8, 9, 10, 11, 12, 13, 14, 15, 0, 1, 2, 3,
                          16, 17, 18, 6, 7, 0, 1, 2, 3, 4, 5, 8, 9, 10, 11, 12};
constexpr int AVAIL[16] = {20, 23, 26, 29, 32, 35, 36, 36, 40, 44, 48, 52, 56, 56, 56, 56};
constexpr int NFILL[9] = {FILL};
constexpr unsigned slot(int n) { return (unsigned)SLOT[n] * H; }
constexpr int ustart(int s) { return s < 8 ? 3 * s : 24 + 4 * (s - 8); }
constexpr int uend(int s) { return ustart(s + 1); }
constexpr int cmin(int a, int b) { return a < b ? a : b; }
constexpr int cmax(int a, int b) { return a > b ? a : b; }
constexpr int issued(int s) { return s < 0 ? INITN : cmax(INITN, cmin(AVAIL[s], s < 7 ? uend(s) + CAPA : s == 7 ? M7 : 99)); }
constexpr int filled(int s) { return s < 7 ? 0 : NFILL[s - 7]; }
constexpr int when(int n) { int s = -1; while (issued(s) <= n) ++s; return s; }
constexpr unsigned QIM = 16 * H, SMALL = 19 * H, RX = 4 * H, RY = 8 * H, OIM = 14 * H, POIM = 13 * H;
constexpr unsigned WS_O = 0, WS_P = 6u << 20, WS_F = 16u << 20;
constexpr float C = 0.08838834764831845f * 1.4426950408889634f;

template <int V> struct IC { static constexpr int v = V; };
template <int A, int B, class F> DEV void sfor(F&& f) {
  if constexpr (A < B) { f(IC<A>{}); sfor<A + 1, B>(f); }
}

struct Ld {
  __amdgpu_buffer_rsrc_t sX, sK, sV, sWq, sWo;
  unsigned ldsb, vx0, vx1, vwq, vk, vv, vwo, xb, wqb, kvb, wob, rotA, g, hd[4];
};
template <int M> DEV void issue1(const Ld& L) {
  constexpr int N = M / 2, P = M % 2;
  const unsigned d = L.ldsb + slot(N) + P * 1024;
  if constexpr (N < 24) {
    constexpr int j = N / 3, r = N % 3;
    const unsigned jr = (j + L.rotA) & 7;
    if constexpr (r == 0) dma(d, P ? L.vx1 : L.vx0, L.sX, L.xb + jr * 128 + P * 8192);
    else dma(d, L.vwq, L.sWq, L.wqb + (64 * jr + 32 * (r - 1) + 4 * P) * 8192);
  } else {
    constexpr int u = ((N - 24) / 4) & 3, j = (N - 24) % 4;
    const unsigned so = L.kvb + (128 * ((u + L.g) & 3) + 32 * j) * 256 + P * 1024;
    if constexpr (N >= 40) dma(d, L.vv, L.sV, so);
    else dma(d, L.vk ^ (P * 64), L.sK, so);
  }
}
template <int A, int B> DEV void issue_hu(const Ld& L) { sfor<2 * A, 2 * B>([&](auto M) { issue1<M.v>(L); }); }
#define VWAIT(S) do { static_assert(issued((S) - 1) >= uend(S), "unit not issued"); \
  asm volatile("s_waitcnt vmcnt(%0)" ::"i"(2 * (issued((S) - 1) - uend(S) + filled((S) - 1) - filled(when(uend(S) - 1) - 1))) : "memory"); } while (0)

__global__ __launch_bounds__(768, 3) KATTR void gqa_fused(const void* __restrict__ x, const void* __restrict__ kc, const void* __restrict__ vc,
                                                    const void* __restrict__ wq, const void* __restrict__ wo, void* __restrict__ out, char* ws) {
  extern __shared__ __attribute__((aligned(16))) char smem[];
  LAS char* lds = (LAS char*)smem;
  const int wid = blockIdx.x, kvh = wid & 7, g = (wid >> 3) & 3, b = wid >> 5, h = kvh * 4 + g;
  const int wave = __builtin_amdgcn_readfirstlane(threadIdx.x >> 6), lane = threadIdx.x & 63;
  u64* epoch = (u64*)(ws + WS_F) + b * 16;
  const __amdgpu_buffer_rsrc_t sW = __builtin_amdgcn_make_buffer_rsrc(ws, 0, WS_F, 0x00020000);
  const unsigned pgrp = WS_P + (unsigned)(b * 4 + g) * 163840u;
  const int w = wave & 3, hf = lane >> 5, l32 = lane & 31, e = w & 1, kq = (wave >> 1) & 3, er = (wave >> 2) & 1;
  const unsigned q4 = (lane & 15) >> 2, p4 = lane & 3, blk = (lane >> 4) & 1;
  LAS char* lin = lds + lane * 16;
  LAS char* mxr = lds + SMALL + e * 512 + l32 * 16;
  STN();
  const int pb = b % 3;
  auto phead = [&](int p) { const int r = p + pb; return (g + 1 + (r >= 3 ? r - 3 : r)) & 3; };
  if (wave >= 8) {
    Ld L;
    L.sX = __builtin_amdgcn_make_buffer_rsrc((void*)x, 0, 8u * 64 * 512 * 2, 0x00020000);
    L.sWq = __builtin_amdgcn_make_buffer_rsrc((void*)wq, 0, 4u << 20, 0x00020000);
    L.ldsb = (unsigned)(size_t)lds + w * 2048;
    const unsigned r4 = lane >> 4, c16 = lane & 15, swT = (c16 ^ (r4 << 2)) * 16;
    L.vx0 = (lane >> 3) * 1024 + (((lane & 7) ^ r4) * 16);
    L.vx1 = (lane >> 3) * 1024 + (((lane & 7) ^ (4 + r4)) * 16);
    L.vwq = r4 * 8192 + swT;
    L.xb = b * 65536 + w * 16 * 1024;
    L.wqb = h * 256 + w * 8 * 8192;
    L.rotA = b + 2 * g; L.g = g;
    issue_hu<0, INITN>(L);
    L.sK = __builtin_amdgcn_make_buffer_rsrc((void*)kc, 0, 8u << 20, 0x00020000);
    L.sV = __builtin_amdgcn_make_buffer_rsrc((void*)vc, 0, 8u << 20, 0x00020000);
    L.sWo = __builtin_amdgcn_make_buffer_rsrc((void*)wo, 0, 4u << 20, 0x00020000);
    L.vv = r4 * 256 + swT; L.vwo = r4 * 1024 + swT;
    L.vk = r4 * 256 + ((c16 ^ (8 * (w & 1) + r4)) * 16);
    L.kvb = (unsigned)(b * 8 + kvh) * 131072u + w * 2048;
    L.wob = kvh * 524288 + g * 256 + w * 8 * 1024;
    L.hd[0] = L.wob + g * 131072;
#pragma unroll
    for (int p = 0; p < 3; ++p) L.hd[p + 1] = L.wob + phead(p) * 131072;
    u32x4 wr[32];
    sfor<0, 16>([&](auto S) {
      VWAIT(S.v);
      BAR();
      issue_hu<issued(S.v - 1), issued(S.v)>(L);
      sfor<2 * filled(S.v - 1), 2 * filled(S.v)>([&](auto M) {
        wr[M.v] = __builtin_amdgcn_raw_buffer_load_b128(L.sWo, L.vwo, L.hd[M.v >> 3] + (32 * ((M.v >> 1) & 3) + 4 * (M.v & 1)) * 1024, 0);
      });
      asm volatile("" ::: "memory");
    });
    LAS char* wd[3];
    wd[0] = lin + w * 2048; wd[1] = wd[0] + 8 * H; wd[2] = wd[0] + 16 * H;
    PIN(wd[0]); PIN(wd[1]); PIN(wd[2]);
    sfor<0, 32>([&](auto M) {
      constexpr int sl = SLOT[56 + M.v / 2];
      if constexpr (M.v == 10 || M.v == 22) BAR_C();
      *(LAS u32x4*)(wd[sl >> 3] + (sl & 7) * H + (M.v & 1) * 1024) = wr[M.v];
    });
    BAR_C();
    BAR();
    return;
  }
  bf16x8 qf[8];
  f32x16 O[4];
  float mloc;
  const unsigned trb = 256 * (4 * hf + q4) + 8 * (p4 & 1), trx = 2 * blk + (p4 >> 1);
  auto attn = [&](auto ZC) {
    constexpr int Z = ZC.v;
    auto bar = [&](auto R, bool drain) {
      if (drain) BAR_C(); else BAR();
    };
    LAS char* ka[8];
#pragma unroll
    for (int ks = 0; ks < 8; ++ks) { ka[ks] = lds + slot(24 + 8 * Z) + (w >> 1) * 2 * H + 256 * l32 + 16 * ((2 * ks + hf) ^ (l32 & 15)); PIN(ka[ks]); }
    bf16x8 kf[32];
    f32x16 S[4];
    float mt[4];
    auto tmax = [&](const f32x16& a) {
      float m = fmax2(fmax2(a[0], a[1]), a[2]);
#pragma unroll
      for (int r = 3; r < 15; r += 2) m = fmax2(fmax2(m, a[r]), a[r + 1]);
      return fmax2(m, a[15]);
    };
    sfor<0, 36>([&](auto T) {
      constexpr int t = T.v;
      if constexpr (t == 0 && Z) bar(IC<0>{}, false);
      if constexpr (t == 16) bar(IC<1>{}, false);
      if constexpr (t == 30) bar(IC<2>{}, false);
      if constexpr ((t & 15) < 8 && t < 32) {
        constexpr int i = (t & 16) + 2 * (t & 7);
        kf[i] = ld128(ka[i & 7] + (Z ? 0 : (i >> 4) * 4 * H) + ((i >> 3) & 1) * H);
        kf[i + 1] = ld128(ka[(i + 1) & 7] + (Z ? 0 : (i >> 4) * 4 * H) + ((i >> 3) & 1) * H);
      }
      if constexpr (Z && t >= 8 && t < 16) { ka[t - 8] -= slot(32) - slot(36); PIN(ka[t - 8]); }
      if constexpr (!Z && t < 4) { qf[2 * t] = ld128(lin + QIM + e * H + 2 * t * 1024); qf[2 * t + 1] = ld128(lin + QIM + e * H + (2 * t + 1) * 1024); }
      if constexpr (t >= 4) S[(t - 4) >> 3] = mfma(kf[t - 4], qf[(t - 4) & 7], ((t - 4) & 7) == 0 ? f32x16{} : S[(t - 4) >> 3]);
      if constexpr (t == 15 || t == 23 || t == 31) mt[(t - 15) >> 3] = tmax(S[(t - 15) >> 3]);
      SB();
    });
    {
      const float m = fmax2(fmax2(mt[0], mt[1]), fmax2(mt[2], tmax(S[3])));
      u32x2 sw = __builtin_amdgcn_permlane32_swap(f2u(m), f2u(m), false, false);
      mloc = fmax2(u2f(sw.x), u2f(sw.y));
      if (hf == 0) *(LAS float*)(mxr + kq * 4) = mloc;
    }
    {
      const unsigned hi = f2u(-mloc) & 0xffff0000u;
      const float r1 = -mloc - u2f(hi);
      const unsigned mid = f2u(r1) & 0xffff0000u;
      const float lo = r1 - u2f(mid);
      const bf16x8 mb = __builtin_bit_cast(bf16x8, u32x4{mid | (hi >> 16), f2u(lo) >> 16, 0u, 0u});
      const bf16x8 ma = __builtin_bit_cast(bf16x8, u32x4{hf ? 0u : 0x3f803f80u, hf ? 0u : 0x3f80u, 0u, 0u});
#pragma unroll
      for (int i = 0; i < 4; ++i) S[i] = mfma(ma, mb, S[i]);
      SB();
    }
    unsigned pp[32];
    f32x4 ls = {};
    const unsigned one = ((lane >> 4) ^ lane) & 1 ? 0u : 0x3f803f80u;
    const bf16x8 sel = __builtin_bit_cast(bf16x8, u32x4{one, one, one, one});
    LAS char* va[4];
    sfor<0, 33>([&](auto T) {
      constexpr int t = T.v;
      if constexpr (t == E3) bar(IC<3>{}, true);
      if constexpr (t < 32) {
        pp[t] = pk(ex2(S[t >> 3][2 * (t & 7)]), ex2(S[t >> 3][2 * (t & 7) + 1]));
      }
      if constexpr (t >= 4 && (t & 3) == 0)
        ls = __builtin_amdgcn_mfma_f32_16x16x32_bf16(sel, __builtin_bit_cast(bf16x8, u32x4{pp[t - 4], pp[t - 3], pp[t - 2], pp[t - 1]}), ls, 0, 0, 0);
      if constexpr (t >= 28 && t < 32) { va[t - 28] = lds + slot(40 + 8 * Z) + (w >> 1) * 2 * H + trb + 16 * ((4 * ((kq + t - 27) & 3) + trx) ^ (q4 << 2)); PIN(va[t - 28]); }
      SB();
    });
    bf16x8 vf[32];
    LAS char* ow = Z ? lin + RY + (e * 6 + (kq & 1) * 3) * 4096 : lin + RX + e * 4096;
    auto put_tile = [&](LAS char* p, const f32x16& o) {
#pragma unroll
      for (int q = 0; q < 4; ++q) *(LAS f32x4*)(p + q * 1024) = f32x4{o[4 * q], o[4 * q + 1], o[4 * q + 2], o[4 * q + 3]};
    };
    sfor<0, 38>([&](auto T) {
      constexpr int t = T.v;
      if constexpr (t == 0) bar(IC<4>{}, false);
      if constexpr (t == 16) bar(IC<5>{}, false);
      if constexpr (t < 32) vf[t] = ldtr(va[(t >> 1) & 3] + (Z ? 0 : (t >> 4) * 4 * H) + ((t >> 3) & 1) * H + (t & 1) * 4096, 2048);
      if constexpr (Z && t >= 9 && t < 16 && (t & 1)) { va[(t >> 1) & 3] -= slot(48) - slot(52); PIN(va[(t >> 1) & 3]); }
      if constexpr (t >= 4 && t < 36) {
        constexpr int i = t - 4, p = (i >> 3) * 8 + (i & 1) * 4;
        const u32x4 pv = {pp[p], pp[p + 1], pp[p + 2], pp[p + 3]};
        O[(i >> 1) & 3] = mfma(vf[i], __builtin_bit_cast(bf16x8, pv), i < 8 && !(i & 1) ? f32x16{} : O[(i >> 1) & 3]);
      }
      if constexpr (t == 2) { if (hf == 0) *(LAS float*)(mxr + 1024 + kq * 4) = blk ? ls.y : ls.x; }
      if constexpr (t == 32 || t == 34 || t == 36) {
        constexpr int j = (t - 32) >> 1;
        if constexpr (Z) put_tile(ow + j * 4096, O[j]);
        else if constexpr (j != 1) put_tile(ow + (((kq + 1 + j) & 3) ^ 2) * 8192, O[j]);
      }
      SB();
    });
  };
  auto get_tile = [&](LAS char* p, f32x4* v) {
#pragma unroll
    for (int q = 0; q < 4; ++q) v[q] = *(LAS f32x4*)(p + q * 1024);
  };
  f32x4 ta[4], tb[4], fk;
  auto factors = [&]() {
    const f32x4 mv = *(LAS f32x4*)mxr;
    const float m = max4(mv);
    fk = f32x4{ex2(mv.x - m), ex2(mv.y - m), ex2(mv.z - m), ex2(mv.w - m)};
  };
  float fo, fa, fb;
  {
    u64 ep = __hip_atomic_load(epoch, RLX);
    LAS char *trN[3], *xa[3][4];
#pragma unroll
    for (int r = 0; r < 3; ++r) {
      trN[r] = lds + r * 8 * H + 256 * (8 * hf + q4) + 16 * ((4 * w + trx) ^ (q4 << 2)) + 8 * (p4 & 1); PIN(trN[r]);
#pragma unroll
      for (int ks = 0; ks < 4; ++ks) { xa[r][ks] = lds + r * 8 * H + er * 4096 + 128 * l32 + 16 * ((2 * ks + hf) ^ ((l32 >> 1) & 7)); PIN(xa[r][ks]); }
    }
    f32x16 acc = {};
    bf16x8 fa_[4], fb_[4];
    sfor<0, 35>([&](auto T) {
      constexpr int t = T.v;
      if constexpr (t < 32) {
        constexpr int j = t >> 2, ks = t & 3, sa = SLOT[3 * j + 1 + (ks >> 1)], sx = SLOT[3 * j];
        if constexpr (ks == 0) BAR();
        fa_[t & 3] = ldtr(trN[sa >> 3] + (sa & 7) * H + (ks & 1) * 4096, 1024);
        fb_[t & 3] = ld128(xa[sx >> 3][ks] + (sx & 7) * H);
      }
      if constexpr (t >= 3) acc = mfma(fa_[(t - 3) & 3], fb_[(t - 3) & 3], acc);
      if constexpr (t == 30) {
        PIN(ep);
        if (threadIdx.x == 0) *(LAS u64*)(lds + SMALL + 2048) = ep * 0x9E3779B97F4A7C15ull + 0x632BE59BD9B4E019ull;
      }
      SB();
    });
#pragma unroll
    for (int k = 0; k < 16; ++k) acc[k] *= C;
#pragma unroll
    for (int ks = 0; ks < 2; ++ks) *(LAS u32x4*)(lin + QIM + er * H + (2 * w + ks) * 1024) = pack8(acc, ks);
    BAR_C();
  }
  if (wave >= 4) {
#pragma unroll
    for (int k = 0; k < 8; ++k) qf[k] = ld128(lin + QIM + e * H + k * 1024);
    BAR();
    attn(IC<1>{});
    factors();
    get_tile(lin + RX + ((kq ^ 2) * 2 + e) * 4096, ta);
    BAR_C();
    get_tile(lin + RY + (e * 6 + ((kq & 1) ^ 1) * 3 + ((kq & 1) ? 0 : 2)) * 4096, tb);
    fo = (kq & 1) ? fk.w : fk.z; fa = 1.0f; fb = (kq & 1) ? fk.z : fk.w;
  } else {
    attn(IC<0>{});
    BAR_C();
    factors();
    LAS char* r1 = lin + RX + (kq * 2 + e) * 4096;
    get_tile(r1, ta);
    get_tile(lin + RX + ((kq ^ 2) * 2 + e) * 4096, tb);
    {
      const float fs = kq ? fk.y : fk.x, fr = kq ? fk.x : fk.y;
#pragma unroll
      for (int q = 0; q < 4; ++q) {
        f32x4 v;
#pragma unroll
        for (int k = 0; k < 4; ++k) v[k] = __builtin_fmaf(ta[q][k], fr, O[1][4 * q + k] * fs);
        *(LAS f32x4*)(r1 + q * 1024) = v;
      }
#pragma unroll
      for (int k = 0; k < 16; ++k) O[3][k] = __builtin_fmaf(tb[k >> 2][k & 3], fr, O[3][k] * fs);
    }
    BAR_C();
    BAR();
    get_tile(lin + RY + (e * 6 + ((kq + 1) & 3)) * 4096, ta);
    get_tile(lin + RY + (e * 6 + 3 + kq) * 4096, tb);
    fo = 1.0f; fa = fk.z; fb = fk.w;
  }
  const u32x2 tag = *(LAS u32x2*)(lds + SMALL + 2048);
  const unsigned t0 = tag.x, t1 = tag.y;
  {
    const f32x4 lv = *(LAS f32x4*)(mxr + 1024);
    const float inv = 1.0f / ((lv.x * fk.x + lv.y * fk.y) + (lv.z * fk.z + lv.w * fk.w));
    fo *= inv; fa *= inv; fb *= inv;
    u32x4 o[2];
#pragma unroll
    for (int k = 0; k < 8; ++k) {
      const int r = 2 * k;
      const float v0 = __builtin_fmaf(tb[r >> 2][r & 3], fb, __builtin_fmaf(ta[r >> 2][r & 3], fa, O[3][r] * fo));
      const float v1 = __builtin_fmaf(tb[r >> 2][(r & 3) + 1], fb, __builtin_fmaf(ta[r >> 2][(r & 3) + 1], fa, O[3][r + 1] * fo));
      o[k >> 2][k & 3] = pk(v0, v1);
    }
    *(LAS u32x4*)(lin + OIM + (kq * 4 + e) * 1024) = o[0];
    *(LAS u32x4*)(lin + OIM + (kq * 4 + 2 + e) * 1024) = o[1];
    const unsigned so = WS_O + (unsigned)wid * 20480u + wave * 2560;
    __builtin_amdgcn_raw_buffer_store_b128(o[0], sW, lane * 16, so, 16);
    __builtin_amdgcn_raw_buffer_store_b128(o[1], sW, lane * 16, so + 1024, 16);
    __builtin_amdgcn_raw_buffer_store_b64(seal(o[0], o[1], t0, t1), sW, lane * 8, so + 2048, 16);
  }
  BAR_C();
  f32x16 dd;
  {
    LAS char* wa[3];
    wa[0] = lds + trb + 16 * ((4 * w + trx) ^ (q4 << 2)); wa[1] = wa[0] + 8 * H; wa[2] = wa[0] + 16 * H;
    LAS char* ob = lin + POIM + er * 1024;
    PIN(wa[0]); PIN(wa[1]); PIN(wa[2]); PIN(ob);
    bf16x8 fa_[4], fb_[4];
    u32x4 x0[3], x1[3]; u32x2 xs[3];
    auto fetch = [&](int p) {
      const unsigned so = WS_O + (unsigned)((wid & ~24) | (phead(p) << 3)) * 20480u + wave * 2560;
      x0[p] = __builtin_amdgcn_raw_buffer_load_b128(sW, lane * 16, so, 16);
      x1[p] = __builtin_amdgcn_raw_buffer_load_b128(sW, lane * 16, so + 1024, 16);
      xs[p] = __builtin_amdgcn_raw_buffer_load_b64(sW, lane * 8, so + 2048, 16);
    };
    sfor<0, 38>([&](auto T) {
      constexpr int t = T.v, k = t < 8 ? t : t - 3;
      if constexpr (t == TF) { fetch(0); fetch(1); fetch(2); }
      if constexpr (t == 11) {
        BAR_C();
        unsigned todo = 7;
        for (int it = 0; it < (1 << 16); ++it) {
#pragma unroll
          for (int p = 0; p < 3; ++p)
            if (todo & (1 << p)) {
              const u32x2 c = seal(x0[p], x1[p], t0, t1);
              if (__builtin_amdgcn_ballot_w64(c.x != xs[p].x || c.y != xs[p].y) == 0) {
                *(LAS u32x4*)(lin + POIM + p * 2 * H + (kq * 4 + e) * 1024) = x0[p];
                *(LAS u32x4*)(lin + POIM + p * 2 * H + (kq * 4 + 2 + e) * 1024) = x1[p];
                todo &= ~(1 << p);
              } else {
                asm volatile("" ::: "memory");
                fetch(p);
              }
            }
          if (!todo) break;
        }
        BAR_C();
      }
      if constexpr (t < 8 || (t >= 11 && t < 35)) {
        fa_[k & 3] = ldtr(wa[SLOT[56 + (k >> 1)] >> 3] + (SLOT[56 + (k >> 1)] & 7) * H + (k & 1) * 4096, 2048);
        fb_[k & 3] = ld128(ob + (k < 8 ? OIM - POIM : ((k >> 3) - 1) * 2 * H) + (k & 7) * 2048);
      }
      if constexpr ((t >= 3 && t < 11) || t >= 14) {
        constexpr int km = t < 11 ? t - 3 : t - 6;
        dd = mfma(fa_[km & 3], fb_[km & 3], km == 0 ? f32x16{} : dd);
      }
      SB();
    });
  }
  STN();
  {
    const unsigned vo = (l32 >> 3) * 20480 + ((l32 >> 2) & 1) * 128 + (hf * 4 + (l32 & 3)) * 16;
    const unsigned vc2 = (l32 >> 3) * 20480 + ((l32 >> 2) & 1) * 64 + (hf * 4 + (l32 & 3)) * 8;
    const unsigned so = pgrp + er * 81920 + kvh * 2560;
    const u32x4 a = pack8(dd, 0), c = pack8(dd, 1);
    __builtin_amdgcn_raw_buffer_store_b128(a, sW, vo, so + w * 256, 16);
    __builtin_amdgcn_raw_buffer_store_b128(c, sW, vo, so + 1024 + w * 256, 16);
    __builtin_amdgcn_raw_buffer_store_b64(seal(a, c, t0, t1), sW, vc2, so + 2048 + w * 128, 16);
  }
  STN();
  {
    const unsigned vo = (lane >> 3) * 2560 + (lane & 7) * 16, vc2 = (lane >> 3) * 2560 + (lane & 7) * 8;
    const unsigned so = pgrp + kvh * 20480;
    u32x4 v0, v1; u32x2 cs;
    for (int it = 0; it < (1 << 16); ++it) {
      v0 = __builtin_amdgcn_raw_buffer_load_b128(sW, vo, so + (w * 2 + er) * 128, 16);
      v1 = __builtin_amdgcn_raw_buffer_load_b128(sW, vo, so + 1024 + (w * 2 + er) * 128, 16);
      cs = __builtin_amdgcn_raw_buffer_load_b64(sW, vc2, so + 2048 + (w * 2 + er) * 64, 16);
      const u32x2 c0 = seal(v0, v1, t0, t1);
      if (__builtin_amdgcn_ballot_w64(c0.x != cs.x || c0.y != cs.y) == 0) break;
      asm volatile("" ::: "memory");
    }
    if ((wid & 31) == 0 && wave == 0 && lane == 0) __hip_atomic_store(epoch, __builtin_bit_cast(u64, tag), RLX);
    float c8[8], c4[4];
#pragma unroll
    for (int k = 0; k < 8; ++k) {
      const unsigned a0 = v0[k >> 1], b0 = v1[k >> 1];
      u32x2 sw = __builtin_amdgcn_permlane32_swap((k & 1) ? a0 & 0xffff0000u : a0 << 16, (k & 1) ? b0 & 0xffff0000u : b0 << 16, false, false);
      c8[k] = u2f(sw.x) + u2f(sw.y);
    }
#pragma unroll
    for (int k = 0; k < 4; ++k) {
      u32x2 sw = __builtin_amdgcn_permlane16_swap(f2u(c8[k]), f2u(c8[k + 4]), false, false);
      const float s = u2f(sw.x) + u2f(sw.y);
      c4[k] = s + u2f(__builtin_amdgcn_update_dpp(0, f2u(s), 0x128, 0xf, 0xf, false));
    }
    const bool up = lane & 8;
    const unsigned s = 8 * kvh + 4 * er + (lane & 3);
    const unsigned n = 128 * g + 32 * w + 16 * hf + 8 * ((lane >> 2) & 1) + 4 * ((lane >> 4) & 1) + 2 * ((lane >> 3) & 1);
    *(unsigned*)((char*)out + ((size_t)(b * 64 + s) * 512 + n) * 2) = pk(up ? c4[2] : c4[0], up ? c4[3] : c4[1]);
  }
}

#ifdef __HIP_DEVICE_COMPILE__
#pragma clang attribute pop
#endif
extern "C" void kernel_launch(void* const* d_in, const int* in_sizes, int n_in, void* d_out, int out_size, void* d_ws, size_t ws_size,
                              hipStream_t stream) {
  (void)hipFuncSetAttribute((const void*)gqa_fused, hipFuncAttributeMaxDynamicSharedMemorySize, 163840);
  gqa_fused<<<256, 768, 163840, stream>>>(d_in[0], d_in[1], d_in[2], d_in[3], d_in[4], d_out, (char*)d_ws);
}
```

```cpp
#define PADK 99
#define PADW 0
#define PADWG 128
#include <hip/hip_runtime.h>
#include <hip/hip_bf16.h>

typedef __attribute__((ext_vector_type(8))) __bf16 bf16x8;
typedef __attribute__((ext_vector_type(2))) __bf16 bf16x2;
typedef __attribute__((ext_vector_type(4))) short s16x4;
typedef __attribute__((ext_vector_type(8))) short s16x8;
typedef __attribute__((ext_vector_type(16))) float f32x16;
typedef __attribute__((ext_vector_type(4))) float f32x4;
typedef __attribute__((ext_vector_type(4))) unsigned u32x4;
typedef __attribute__((ext_vector_type(2))) unsigned u32x2;
typedef unsigned long long u64;
#ifdef __HIP_DEVICE_COMPILE__
#pragma clang attribute push(__attribute__((target("no-packed-fp32-ops"))), apply_to = function)
#endif
#define LAS __attribute__((address_space(3)))
#define DEV __device__ __forceinline__

#define STN() do { if (stc == 0) tstart = __builtin_amdgcn_s_memrealtime(); else if (stc == PADK && wave == PADW) tk = __builtin_amdgcn_s_memrealtime(); ++stc; } while (0)
#define PADEND() do { STN(); if (PADWG < 0 || blockIdx.x == PADWG) { unsigned long long lim = tstart + 4000 + (tk ? tk - tstart : 0); for (int it = 0; it < 100000; ++it) { if (__builtin_amdgcn_s_memrealtime() >= lim) break; __builtin_amdgcn_s_sleep(2); } } } while (0)
#define KATTR

#define CAPA 8
#define INITN 9
#define E3 10
#define TF 8
#define M7 36
#define FILL 0, 2, 4, 6, 8, 10, 16, 16, 16

constexpr unsigned H = 8192;
#define BAR() do { STN(); asm volatile("s_barrier" ::: "memory"); STN(); } while (0)
#define BAR_C() do { asm volatile("s_waitcnt lgkmcnt(0)" ::: "memory"); BAR(); } while (0)
#define SB() __builtin_amdgcn_sched_barrier(0)
#define PIN(p) asm volatile("" : "+v"(p))
#define RLX __ATOMIC_RELAXED, __HIP_MEMORY_SCOPE_AGENT

DEV unsigned f2u(float f) { return __builtin_bit_cast(unsigned, f); }
DEV float u2f(unsigned u) { return __builtin_bit_cast(float, u); }
DEV void dma(unsigned m0v, unsigned voff, __amdgpu_buffer_rsrc_t srd, unsigned soff) {
  asm volatile("s_mov_b32 m0, %0\n\ts_nop 0\n\tbuffer_load_dwordx4 %1, %2, %3 offen lds" ::"s"(m0v), "v"(voff), "s"(srd), "s"(soff) : "memory");
}
DEV bf16x8 ld128(LAS char* p) { return *(LAS bf16x8*)p; }
DEV bf16x8 ldtr(LAS char* p, unsigned tstep) {
  s16x4 a = __builtin_amdgcn_ds_read_tr16_b64_v4i16((LAS s16x4*)p);
  s16x4 b = __builtin_amdgcn_ds_read_tr16_b64_v4i16((LAS s16x4*)(p + tstep));
  s16x8 r = __builtin_shufflevector(a, b, 0, 1, 2, 3, 4, 5, 6, 7);
  return __builtin_bit_cast(bf16x8, r);
}
DEV unsigned pk(float a, float b) { bf16x2 r = {(__bf16)a, (__bf16)b}; return __builtin_bit_cast(unsigned, r); }
DEV f32x16 mfma(bf16x8 a, bf16x8 b, f32x16 c) { return __builtin_amdgcn_mfma_f32_32x32x16_bf16(a, b, c, 0, 0, 0); }
DEV float fmax2(float a, float b) { return __builtin_fmaxf(a, b); }
DEV float max4(f32x4 v) { return fmax2(fmax2(v.x, v.y), fmax2(v.z, v.w)); }
DEV float ex2(float a) { return __builtin_amdgcn_exp2f(a); }
DEV u32x4 pack8(const f32x16& a, int ks) {
  unsigned r0 = pk(a[8 * ks + 0], a[8 * ks + 1]), r1 = pk(a[8 * ks + 2], a[8 * ks + 3]);
  unsigned r2 = pk(a[8 * ks + 4], a[8 * ks + 5]), r3 = pk(a[8 * ks + 6], a[8 * ks + 7]);
  u32x2 s0 = __builtin_amdgcn_permlane32_swap(r0, r2, false, false);
  u32x2 s1 = __builtin_amdgcn_permlane32_swap(r1, r3, false, false);
  return u32x4{s0.x, s1.x, s0.y, s1.y};
}
DEV unsigned rot(unsigned v, int r) { return __builtin_amdgcn_alignbit(v, v, r); }
DEV u32x2 seal(u32x4 a, u32x4 b, unsigned t0, unsigned t1) {
  const unsigned s = (a.x + a.y + a.z) + (a.w + b.x + b.y) + (b.z + b.w + t0);
  const unsigned x = (a.x ^ rot(a.y, 3) ^ rot(a.z, 7)) ^ (rot(a.w, 11) ^ rot(b.x, 13) ^ rot(b.y, 17)) ^ (rot(b.z, 23) ^ rot(b.w, 29) ^ t1);
  return u32x2{s, x};
}

constexpr int SLOT[72] = {0, 1, 2, 3, 4, 5, 6, 7, 8, 9, 10, 11, 12, 13, 14, 15, 16, 17, 18, 19, 0, 1, 2, 3,
                          4, 5, 6, 7, 8, 9, 10, 11, 12, 13, 14, 15, 0, 1, 2, 3,
                          4, 5, 6, 7, 8, 9, 10, 11, 12, 13, 14, 15, 0, 1, 2, 3,
                          16, 17, 18, 6, 7, 0, 1, 2, 3, 4, 5, 8, 9, 10, 11, 12};
constexpr int AVAIL[16] = {20, 23, 26, 29, 32, 35, 36, 36, 40, 44, 48, 52, 56, 56, 56, 56};
constexpr int NFILL[9] = {FILL};
constexpr unsigned slot(int n) { return (unsigned)SLOT[n] * H; }
constexpr int ustart(int s) { return s < 8 ? 3 * s : 24 + 4 * (s - 8); }
constexpr int uend(int s) { return ustart(s + 1); }
constexpr int cmin(int a, int b) { return a < b ? a : b; }
constexpr int cmax(int a, int b) { return a > b ? a : b; }
constexpr int issued(int s) { return s < 0 ? INITN : cmax(INITN, cmin(AVAIL[s], s < 7 ? uend(s) + CAPA : s == 7 ? M7 : 99)); }
constexpr int filled(int s) { return s < 7 ? 0 : NFILL[s - 7]; }
constexpr int when(int n) { int s = -1; while (issued(s) <= n) ++s; return s; }
constexpr unsigned QIM = 16 * H, SMALL = 19 * H, RX = 4 * H, RY = 8 * H, OIM = 14 * H, POIM = 13 * H;
constexpr unsigned WS_O = 0, WS_P = 6u << 20, WS_F = 16u << 20;
constexpr float C = 0.08838834764831845f * 1.4426950408889634f;

template <int V> struct IC { static constexpr int v = V; };
template <int A, int B, class F> DEV void sfor(F&& f) {
  if constexpr (A < B) { f(IC<A>{}); sfor<A + 1, B>(f); }
}

struct Ld {
  __amdgpu_buffer_rsrc_t sX, sK, sV, sWq, sWo;
  unsigned ldsb, vx0, vx1, vwq, vk, vv, vwo, xb, wqb, kvb, wob, rotA, g, hd[4];
};
template <int M> DEV void issue1(const Ld& L) {
  constexpr int N = M / 2, P = M % 2;
  const unsigned d = L.ldsb + slot(N) + P * 1024;
  if constexpr (N < 24) {
    constexpr int j = N / 3, r = N % 3;
    const unsigned jr = (j + L.rotA) & 7;
    if constexpr (r == 0) dma(d, P ? L.vx1 : L.vx0, L.sX, L.xb + jr * 128 + P * 8192);
    else dma(d, L.vwq, L.sWq, L.wqb + (64 * jr + 32 * (r - 1) + 4 * P) * 8192);
  } else {
    constexpr int u = ((N - 24) / 4) & 3, j = (N - 24) % 4;
    const unsigned so = L.kvb + (128 * ((u + L.g) & 3) + 32 * j) * 256 + P * 1024;
    if constexpr (N >= 40) dma(d, L.vv, L.sV, so);
    else dma(d, L.vk ^ (P * 64), L.sK, so);
  }
}
template <int A, int B> DEV void issue_hu(const Ld& L) { sfor<2 * A, 2 * B>([&](auto M) { issue1<M.v>(L); }); }
#define VWAIT(S) do { static_assert(issued((S) - 1) >= uend(S), "unit not issued"); \
  asm volatile("s_waitcnt vmcnt(%0)" ::"i"(2 * (issued((S) - 1) - uend(S) + filled((S) - 1) - filled(when(uend(S) - 1) - 1))) : "memory"); } while (0)

__global__ __launch_bounds__(768, 3) KATTR void gqa_fused(const void* __restrict__ x, const void* __restrict__ kc, const void* __restrict__ vc,
                                                    const void* __restrict__ wq, const void* __restrict__ wo, void* __restrict__ out, char* ws) {
  extern __shared__ __attribute__((aligned(16))) char smem[];
  LAS char* lds = (LAS char*)smem;
  const int wid = blockIdx.x, kvh = wid & 7, g = (wid >> 3) & 3, b = wid >> 5, h = kvh * 4 + g;
  const int wave = __builtin_amdgcn_readfirstlane(threadIdx.x >> 6), lane = threadIdx.x & 63;
  u64* epoch = (u64*)(ws + WS_F) + b * 16;
  const __amdgpu_buffer_rsrc_t sW = __builtin_amdgcn_make_buffer_rsrc(ws, 0, WS_F, 0x00020000);
  const unsigned pgrp = WS_P + (unsigned)(b * 4 + g) * 163840u;
  const int w = wave & 3, hf = lane >> 5, l32 = lane & 31, e = w & 1, kq = (wave >> 1) & 3, er = (wave >> 2) & 1;
  const unsigned q4 = (lane & 15) >> 2, p4 = lane & 3, blk = (lane >> 4) & 1;
  LAS char* lin = lds + lane * 16;
  LAS char* mxr = lds + SMALL + e * 512 + l32 * 16;
  int stc = 0; unsigned long long tstart = 0, tk = 0;
  STN();
  const int pb = b % 3;
  auto phead = [&](int p) { const int r = p + pb; return (g + 1 + (r >= 3 ? r - 3 : r)) & 3; };
  if (wave >= 8) {
    Ld L;
    L.sX = __builtin_amdgcn_make_buffer_rsrc((void*)x, 0, 8u * 64 * 512 * 2, 0x00020000);
    L.sWq = __builtin_amdgcn_make_buffer_rsrc((void*)wq, 0, 4u << 20, 0x00020000);
    L.ldsb = (unsigned)(size_t)lds + w * 2048;
    const unsigned r4 = lane >> 4, c16 = lane & 15, swT = (c16 ^ (r4 << 2)) * 16;
    L.vx0 = (lane >> 3) * 1024 + (((lane & 7) ^ r4) * 16);
    L.vx1 = (lane >> 3) * 1024 + (((lane & 7) ^ (4 + r4)) * 16);
    L.vwq = r4 * 8192 + swT;
    L.xb = b * 65536 + w * 16 * 1024;
    L.wqb = h * 256 + w * 8 * 8192;
    L.rotA = b + 2 * g; L.g = g;
    issue_hu<0, INITN>(L);
    L.sK = __builtin_amdgcn_make_buffer_rsrc((void*)kc, 0, 8u << 20, 0x00020000);
    L.sV = __builtin_amdgcn_make_buffer_rsrc((void*)vc, 0, 8u << 20, 0x00020000);
    L.sWo = __builtin_amdgcn_make_buffer_rsrc((void*)wo, 0, 4u << 20, 0x00020000);
    L.vv = r4 * 256 + swT; L.vwo = r4 * 1024 + swT;
    L.vk = r4 * 256 + ((c16 ^ (8 * (w & 1) + r4)) * 16);
    L.kvb = (unsigned)(b * 8 + kvh) * 131072u + w * 2048;
    L.wob = kvh * 524288 + g * 256 + w * 8 * 1024;
    L.hd[0] = L.wob + g * 131072;
#pragma unroll
    for (int p = 0; p < 3; ++p) L.hd[p + 1] = L.wob + phead(p) * 131072;
    u32x4 wr[32];
    sfor<0, 16>([&](auto S) {
      VWAIT(S.v);
      BAR();
      issue_hu<issued(S.v - 1), issued(S.v)>(L);
      sfor<2 * filled(S.v - 1), 2 * filled(S.v)>([&](auto M) {
        wr[M.v] = __builtin_amdgcn_raw_buffer_load_b128(L.sWo, L.vwo, L.hd[M.v >> 3] + (32 * ((M.v >> 1) & 3) + 4 * (M.v & 1)) * 1024, 0);
      });
      asm volatile("" ::: "memory");
    });
    LAS char* wd[3];
    wd[0] = lin + w * 2048; wd[1] = wd[0] + 8 * H; wd[2] = wd[0] + 16 * H;
    PIN(wd[0]); PIN(wd[1]); PIN(wd[2]);
    sfor<0, 32>([&](auto M) {
      constexpr int sl = SLOT[56 + M.v / 2];
      if constexpr (M.v == 10 || M.v == 22) BAR_C();
      *(LAS u32x4*)(wd[sl >> 3] + (sl & 7) * H + (M.v & 1) * 1024) = wr[M.v];
    });
    BAR_C();
    BAR();
    PADEND();
    return;
  }
  bf16x8 qf[8];
  f32x16 O[4];
  float mloc;
  const unsigned trb = 256 * (4 * hf + q4) + 8 * (p4 & 1), trx = 2 * blk + (p4 >> 1);
  auto attn = [&](auto ZC) {
    constexpr int Z = ZC.v;
    auto bar = [&](auto R, bool drain) {
      if (drain) BAR_C(); else BAR();
    };
    LAS char* ka[8];
#pragma unroll
    for (int ks = 0; ks < 8; ++ks) { ka[ks] = lds + slot(24 + 8 * Z) + (w >> 1) * 2 * H + 256 * l32 + 16 * ((2 * ks + hf) ^ (l32 & 15)); PIN(ka[ks]); }
    bf16x8 kf[32];
    f32x16 S[4];
    float mt[4];
    auto tmax = [&](const f32x16& a) {
      float m = fmax2(fmax2(a[0], a[1]), a[2]);
#pragma unroll
      for (int r = 3; r < 15; r += 2) m = fmax2(fmax2(m, a[r]), a[r + 1]);
      return fmax2(m, a[15]);
    };
    sfor<0, 36>([&](auto T) {
      constexpr int t = T.v;
      if constexpr (t == 0 && Z) bar(IC<0>{}, false);
      if constexpr (t == 16) bar(IC<1>{}, false);
      if constexpr (t == 30) bar(IC<2>{}, false);
      if constexpr ((t & 15) < 8 && t < 32) {
        constexpr int i = (t & 16) + 2 * (t & 7);
        kf[i] = ld128(ka[i & 7] + (Z ? 0 : (i >> 4) * 4 * H) + ((i >> 3) & 1) * H);
        kf[i + 1] = ld128(ka[(i + 1) & 7] + (Z ? 0 : (i >> 4) * 4 * H) + ((i >> 3) & 1) * H);
      }
      if constexpr (Z && t >= 8 && t < 16) { ka[t - 8] -= slot(32) - slot(36); PIN(ka[t - 8]); }
      if constexpr (!Z && t < 4) { qf[2 * t] = ld128(lin + QIM + e * H + 2 * t * 1024); qf[2 * t + 1] = ld128(lin + QIM + e * H + (2 * t + 1) * 1024); }
      if constexpr (t >= 4) S[(t - 4) >> 3] = mfma(kf[t - 4], qf[(t - 4) & 7], ((t - 4) & 7) == 0 ? f32x16{} : S[(t - 4) >> 3]);
      if constexpr (t == 15 || t == 23 || t == 31) mt[(t - 15) >> 3] = tmax(S[(t - 15) >> 3]);
      SB();
    });
    {
      const float m = fmax2(fmax2(mt[0], mt[1]), fmax2(mt[2], tmax(S[3])));
      u32x2 sw = __builtin_amdgcn_permlane32_swap(f2u(m), f2u(m), false, false);
      mloc = fmax2(u2f(sw.x), u2f(sw.y));
      if (hf == 0) *(LAS float*)(mxr + kq * 4) = mloc;
    }
    {
      const unsigned hi = f2u(-mloc) & 0xffff0000u;
      const float r1 = -mloc - u2f(hi);
      const unsigned mid = f2u(r1) & 0xffff0000u;
      const float lo = r1 - u2f(mid);
      const bf16x8 mb = __builtin_bit_cast(bf16x8, u32x4{mid | (hi >> 16), f2u(lo) >> 16, 0u, 0u});
      const bf16x8 ma = __builtin_bit_cast(bf16x8, u32x4{hf ? 0u : 0x3f803f80u, hf ? 0u : 0x3f80u, 0u, 0u});
#pragma unroll
      for (int i = 0; i < 4; ++i) S[i] = mfma(ma, mb, S[i]);
      SB();
    }
    unsigned pp[32];
    f32x4 ls = {};
    const unsigned one = ((lane >> 4) ^ lane) & 1 ? 0u : 0x3f803f80u;
    const bf16x8 sel = __builtin_bit_cast(bf16x8, u32x4{one, one, one, one});
    LAS char* va[4];
    sfor<0, 33>([&](auto T) {
      constexpr int t = T.v;
      if constexpr (t == E3) bar(IC<3>{}, true);
      if constexpr (t < 32) {
        pp[t] = pk(ex2(S[t >> 3][2 * (t & 7)]), ex2(S[t >> 3][2 * (t & 7) + 1]));
      }
      if constexpr (t >= 4 && (t & 3) == 0)
        ls = __builtin_amdgcn_mfma_f32_16x16x32_bf16(sel, __builtin_bit_cast(bf16x8, u32x4{pp[t - 4], pp[t - 3], pp[t - 2], pp[t - 1]}), ls, 0, 0, 0);
      if constexpr (t >= 28 && t < 32) { va[t - 28] = lds + slot(40 + 8 * Z) + (w >> 1) * 2 * H + trb + 16 * ((4 * ((kq + t - 27) & 3) + trx) ^ (q4 << 2)); PIN(va[t - 28]); }
      SB();
    });
    bf16x8 vf[32];
    LAS char* ow = Z ? lin + RY + (e * 6 + (kq & 1) * 3) * 4096 : lin + RX + e * 4096;
    auto put_tile = [&](LAS char* p, const f32x16& o) {
#pragma unroll
      for (int q = 0; q < 4; ++q) *(LAS f32x4*)(p + q * 1024) = f32x4{o[4 * q], o[4 * q + 1], o[4 * q + 2], o[4 * q + 3]};
    };
    sfor<0, 38>([&](auto T) {
      constexpr int t = T.v;
      if constexpr (t == 0) bar(IC<4>{}, false);
      if constexpr (t == 16) bar(IC<5>{}, false);
      if constexpr (t < 32) vf[t] = ldtr(va[(t >> 1) & 3] + (Z ? 0 : (t >> 4) * 4 * H) + ((t >> 3) & 1) * H + (t & 1) * 4096, 2048);
      if constexpr (Z && t >= 9 && t < 16 && (t & 1)) { va[(t >> 1) & 3] -= slot(48) - slot(52); PIN(va[(t >> 1) & 3]); }
      if constexpr (t >= 4 && t < 36) {
        constexpr int i = t - 4, p = (i >> 3) * 8 + (i & 1) * 4;
        const u32x4 pv = {pp[p], pp[p + 1], pp[p + 2], pp[p + 3]};
        O[(i >> 1) & 3] = mfma(vf[i], __builtin_bit_cast(bf16x8, pv), i < 8 && !(i & 1) ? f32x16{} : O[(i >> 1) & 3]);
      }
      if constexpr (t == 2) { if (hf == 0) *(LAS float*)(mxr + 1024 + kq * 4) = blk ? ls.y : ls.x; }
      if constexpr (t == 32 || t == 34 || t == 36) {
        constexpr int j = (t - 32) >> 1;
        if constexpr (Z) put_tile(ow + j * 4096, O[j]);
        else if constexpr (j != 1) put_tile(ow + (((kq + 1 + j) & 3) ^ 2) * 8192, O[j]);
      }
      SB();
    });
  };
  auto get_tile = [&](LAS char* p, f32x4* v) {
#pragma unroll
    for (int q = 0; q < 4; ++q) v[q] = *(LAS f32x4*)(p + q * 1024);
  };
  f32x4 ta[4], tb[4], fk;
  auto factors = [&]() {
    const f32x4 mv = *(LAS f32x4*)mxr;
    const float m = max4(mv);
    fk = f32x4{ex2(mv.x - m), ex2(mv.y - m), ex2(mv.z - m), ex2(mv.w - m)};
  };
  float fo, fa, fb;
  {
    u64 ep = __hip_atomic_load(epoch, RLX);
    LAS char *trN[3], *xa[3][4];
#pragma unroll
    for (int r = 0; r < 3; ++r) {
      trN[r] = lds + r * 8 * H + 256 * (8 * hf + q4) + 16 * ((4 * w + trx) ^ (q4 << 2)) + 8 * (p4 & 1); PIN(trN[r]);
#pragma unroll
      for (int ks = 0; ks < 4; ++ks) { xa[r][ks] = lds + r * 8 * H + er * 4096 + 128 * l32 + 16 * ((2 * ks + hf) ^ ((l32 >> 1) & 7)); PIN(xa[r][ks]); }
    }
    f32x16 acc = {};
    bf16x8 fa_[4], fb_[4];
    sfor<0, 35>([&](auto T) {
      constexpr int t = T.v;
      if constexpr (t < 32) {
        constexpr int j = t >> 2, ks = t & 3, sa = SLOT[3 * j + 1 + (ks >> 1)], sx = SLOT[3 * j];
        if constexpr (ks == 0) BAR();
        fa_[t & 3] = ldtr(trN[sa >> 3] + (sa & 7) * H + (ks & 1) * 4096, 1024);
        fb_[t & 3] = ld128(xa[sx >> 3][ks] + (sx & 7) * H);
      }
      if constexpr (t >= 3) acc = mfma(fa_[(t - 3) & 3], fb_[(t - 3) & 3], acc);
      if constexpr (t == 30) {
        PIN(ep);
        if (threadIdx.x == 0) *(LAS u64*)(lds + SMALL + 2048) = ep * 0x9E3779B97F4A7C15ull + 0x632BE59BD9B4E019ull;
      }
      SB();
    });
#pragma unroll
    for (int k = 0; k < 16; ++k) acc[k] *= C;
#pragma unroll
    for (int ks = 0; ks < 2; ++ks) *(LAS u32x4*)(lin + QIM + er * H + (2 * w + ks) * 1024) = pack8(acc, ks);
    BAR_C();
  }
  if (wave >= 4) {
#pragma unroll
    for (int k = 0; k < 8; ++k) qf[k] = ld128(lin + QIM + e * H + k * 1024);
    BAR();
    attn(IC<1>{});
    factors();
    get_tile(lin + RX + ((kq ^ 2) * 2 + e) * 4096, ta);
    BAR_C();
    get_tile(lin + RY + (e * 6 + ((kq & 1) ^ 1) * 3 + ((kq & 1) ? 0 : 2)) * 4096, tb);
    fo = (kq & 1) ? fk.w : fk.z; fa = 1.0f; fb = (kq & 1) ? fk.z : fk.w;
  } else {
    attn(IC<0>{});
    BAR_C();
    factors();
    LAS char* r1 = lin + RX + (kq * 2 + e) * 4096;
    get_tile(r1, ta);
    get_tile(lin + RX + ((kq ^ 2) * 2 + e) * 4096, tb);
    {
      const float fs = kq ? fk.y : fk.x, fr = kq ? fk.x : fk.y;
#pragma unroll
      for (int q = 0; q < 4; ++q) {
        f32x4 v;
#pragma unroll
        for (int k = 0; k < 4; ++k) v[k] = __builtin_fmaf(ta[q][k], fr, O[1][4 * q + k] * fs);
        *(LAS f32x4*)(r1 + q * 1024) = v;
      }
#pragma unroll
      for (int k = 0; k < 16; ++k) O[3][k] = __builtin_fmaf(tb[k >> 2][k & 3], fr, O[3][k] * fs);
    }
    BAR_C();
    BAR();
    get_tile(lin + RY + (e * 6 + ((kq + 1) & 3)) * 4096, ta);
    get_tile(lin + RY + (e * 6 + 3 + kq) * 4096, tb);
    fo = 1.0f; fa = fk.z; fb = fk.w;
  }
  const u32x2 tag = *(LAS u32x2*)(lds + SMALL + 2048);
  const unsigned t0 = tag.x, t1 = tag.y;
  {
    const f32x4 lv = *(LAS f32x4*)(mxr + 1024);
    const float inv = 1.0f / ((lv.x * fk.x + lv.y * fk.y) + (lv.z * fk.z + lv.w * fk.w));
    fo *= inv; fa *= inv; fb *= inv;
    u32x4 o[2];
#pragma unroll
    for (int k = 0; k < 8; ++k) {
      const int r = 2 * k;
      const float v0 = __builtin_fmaf(tb[r >> 2][r & 3], fb, __builtin_fmaf(ta[r >> 2][r & 3], fa, O[3][r] * fo));
      const float v1 = __builtin_fmaf(tb[r >> 2][(r & 3) + 1], fb, __builtin_fmaf(ta[r >> 2][(r & 3) + 1], fa, O[3][r + 1] * fo));
      o[k >> 2][k & 3] = pk(v0, v1);
    }
    *(LAS u32x4*)(lin + OIM + (kq * 4 + e) * 1024) = o[0];
    *(LAS u32x4*)(lin + OIM + (kq * 4 + 2 + e) * 1024) = o[1];
    const unsigned so = WS_O + (unsigned)wid * 20480u + wave * 2560;
    __builtin_amdgcn_raw_buffer_store_b128(o[0], sW, lane * 16, so, 16);
    __builtin_amdgcn_raw_buffer_store_b128(o[1], sW, lane * 16, so + 1024, 16);
    __builtin_amdgcn_raw_buffer_store_b64(seal(o[0], o[1], t0, t1), sW, lane * 8, so + 2048, 16);
  }
  BAR_C();
  f32x16 dd;
  {
    LAS char* wa[3];
    wa[0] = lds + trb + 16 * ((4 * w + trx) ^ (q4 << 2)); wa[1] = wa[0] + 8 * H; wa[2] = wa[0] + 16 * H;
    LAS char* ob = lin + POIM + er * 1024;
    PIN(wa[0]); PIN(wa[1]); PIN(wa[2]); PIN(ob);
    bf16x8 fa_[4], fb_[4];
    u32x4 x0[3], x1[3]; u32x2 xs[3];
    auto fetch = [&](int p) {
      const unsigned so = WS_O + (unsigned)((wid & ~24) | (phead(p) << 3)) * 20480u + wave * 2560;
      x0[p] = __builtin_amdgcn_raw_buffer_load_b128(sW, lane * 16, so, 16);
      x1[p] = __builtin_amdgcn_raw_buffer_load_b128(sW, lane * 16, so + 1024, 16);
      xs[p] = __builtin_amdgcn_raw_buffer_load_b64(sW, lane * 8, so + 2048, 16);
    };
    sfor<0, 38>([&](auto T) {
      constexpr int t = T.v, k = t < 8 ? t : t - 3;
      if constexpr (t == TF) { fetch(0); fetch(1); fetch(2); }
      if constexpr (t == 11) {
        BAR_C();
        unsigned todo = 7;
        for (int it = 0; it < (1 << 16); ++it) {
#pragma unroll
          for (int p = 0; p < 3; ++p)
            if (todo & (1 << p)) {
              const u32x2 c = seal(x0[p], x1[p], t0, t1);
              if (__builtin_amdgcn_ballot_w64(c.x != xs[p].x || c.y != xs[p].y) == 0) {
                *(LAS u32x4*)(lin + POIM + p * 2 * H + (kq * 4 + e) * 1024) = x0[p];
                *(LAS u32x4*)(lin + POIM + p * 2 * H + (kq * 4 + 2 + e) * 1024) = x1[p];
                todo &= ~(1 << p);
              } else {
                asm volatile("" ::: "memory");
                fetch(p);
              }
            }
          if (!todo) break;
        }
        BAR_C();
      }
      if constexpr (t < 8 || (t >= 11 && t < 35)) {
        fa_[k & 3] = ldtr(wa[SLOT[56 + (k >> 1)] >> 3] + (SLOT[56 + (k >> 1)] & 7) * H + (k & 1) * 4096, 2048);
        fb_[k & 3] = ld128(ob + (k < 8 ? OIM - POIM : ((k >> 3) - 1) * 2 * H) + (k & 7) * 2048);
      }
      if constexpr ((t >= 3 && t < 11) || t >= 14) {
        constexpr int km = t < 11 ? t - 3 : t - 6;
        dd = mfma(fa_[km & 3], fb_[km & 3], km == 0 ? f32x16{} : dd);
      }
      SB();
    });
  }
  STN();
  {
    const unsigned vo = (l32 >> 3) * 20480 + ((l32 >> 2) & 1) * 128 + (hf * 4 + (l32 & 3)) * 16;
    const unsigned vc2 = (l32 >> 3) * 20480 + ((l32 >> 2) & 1) * 64 + (hf * 4 + (l32 & 3)) * 8;
    const unsigned so = pgrp + er * 81920 + kvh * 2560;
    const u32x4 a = pack8(dd, 0), c = pack8(dd, 1);
    __builtin_amdgcn_raw_buffer_store_b128(a, sW, vo, so + w * 256, 16);
    __builtin_amdgcn_raw_buffer_store_b128(c, sW, vo, so + 1024 + w * 256, 16);
    __builtin_amdgcn_raw_buffer_store_b64(seal(a, c, t0, t1), sW, vc2, so + 2048 + w * 128, 16);
  }
  STN();
  {
    const unsigned vo = (lane >> 3) * 2560 + (lane & 7) * 16, vc2 = (lane >> 3) * 2560 + (lane & 7) * 8;
    const unsigned so = pgrp + kvh * 20480;
    u32x4 v0, v1; u32x2 cs;
    for (int it = 0; it < (1 << 16); ++it) {
      v0 = __builtin_amdgcn_raw_buffer_load_b128(sW, vo, so + (w * 2 + er) * 128, 16);
      v1 = __builtin_amdgcn_raw_buffer_load_b128(sW, vo, so + 1024 + (w * 2 + er) * 128, 16);
      cs = __builtin_amdgcn_raw_buffer_load_b64(sW, vc2, so + 2048 + (w * 2 + er) * 64, 16);
      const u32x2 c0 = seal(v0, v1, t0, t1);
      if (__builtin_amdgcn_ballot_w64(c0.x != cs.x || c0.y != cs.y) == 0) break;
      asm volatile("" ::: "memory");
    }
    if ((wid & 31) == 0 && wave == 0 && lane == 0) __hip_atomic_store(epoch, __builtin_bit_cast(u64, tag), RLX);
    float c8[8], c4[4];
#pragma unroll
    for (int k = 0; k < 8; ++k) {
      const unsigned a0 = v0[k >> 1], b0 = v1[k >> 1];
      u32x2 sw = __builtin_amdgcn_permlane32_swap((k & 1) ? a0 & 0xffff0000u : a0 << 16, (k & 1) ? b0 & 0xffff0000u : b0 << 16, false, false);
      c8[k] = u2f(sw.x) + u2f(sw.y);
    }
#pragma unroll
    for (int k = 0; k < 4; ++k) {
      u32x2 sw = __builtin_amdgcn_permlane16_swap(f2u(c8[k]), f2u(c8[k + 4]), false, false);
      const float s = u2f(sw.x) + u2f(sw.y);
      c4[k] = s + u2f(__builtin_amdgcn_update_dpp(0, f2u(s), 0x128, 0xf, 0xf, false));
    }
    const bool up = lane & 8;
    const unsigned s = 8 * kvh + 4 * er + (lane & 3);
    const unsigned n = 128 * g + 32 * w + 16 * hf + 8 * ((lane >> 2) & 1) + 4 * ((lane >> 4) & 1) + 2 * ((lane >> 3) & 1);
    *(unsigned*)((char*)out + ((size_t)(b * 64 + s) * 512 + n) * 2) = pk(up ? c4[2] : c4[0], up ? c4[3] : c4[1]);
  }
  PADEND();
}

#ifdef __HIP_DEVICE_COMPILE__
#pragma clang attribute pop
#endif
extern "C" void kernel_launch(void* const* d_in, const int* in_sizes, int n_in, void* d_out, int out_size, void* d_ws, size_t ws_size,
                              hipStream_t stream) {
  (void)hipFuncSetAttribute((const void*)gqa_fused, hipFuncAttributeMaxDynamicSharedMemorySize, 163840);
  gqa_fused<<<256, 768, 163840, stream>>>(d_in[0], d_in[1], d_in[2], d_in[3], d_in[4], d_out, (char*)d_ws);
}
```

```cpp
#include <hip/hip_runtime.h>
#include <hip/hip_bf16.h>

typedef __attribute__((ext_vector_type(8))) __bf16 bf16x8;
typedef __attribute__((ext_vector_type(2))) __bf16 bf16x2;
typedef __attribute__((ext_vector_type(4))) short s16x4;
typedef __attribute__((ext_vector_type(8))) short s16x8;
typedef __attribute__((ext_vector_type(16))) float f32x16;
typedef __attribute__((ext_vector_type(4))) float f32x4;
typedef __attribute__((ext_vector_type(4))) unsigned u32x4;
typedef __attribute__((ext_vector_type(2))) unsigned u32x2;
typedef unsigned long long u64;
#ifdef __HIP_DEVICE_COMPILE__
#pragma clang attribute push(__attribute__((target("no-packed-fp32-ops"))), apply_to = function)
#endif
#define LAS __attribute__((address_space(3)))
#define DEV __device__ __forceinline__

#define STN()
#define KATTR

#define CAPA 8
#define INITN 9
#define E3 10
#define TF 0
#define SA1 16
#define SA2 17
#define LA1 16
#define LA2 17
#define BARE 0
#define G1 0
#define G2 22
#define PT 25
#define G1B 10
#define SPREAD 1
#define EARLYF 1
#define PRIO 0
#define E3Y 6
#define LFIRST 1
#define LOLD 0
#define LPRIO 3
#define ISLP 1
#define LP1 LPRIO
#define ISS 40, 44, 46, 52, 56, 56, 56, 56
#define LP2 0
#define KTOP 1
#define FS 0
#define FSB 1
#define LAG 5
#define DL1 10
#define DL2 0
#define SLP 2
#define LIGHT2 1
#define SPLIT 0
#define NPRE 22
#define PRET 0
#define WACK2 1
#define MASKP 1
#define SL4 2
#define HV1 0
#define M7 34
#define FILL 0, 0, 0, 0, 2, 6, 12, 16, 16

static_assert(G1 <= 6 && G1B <= 10 && G2 >= 8 && G2 <= 22, "Wo slots");
constexpr unsigned H = 8192;
#define BAR() do { STN(); asm volatile("s_barrier" ::: "memory"); STN(); } while (0)
#define BAR_C() do { asm volatile("s_waitcnt lgkmcnt(0)" ::: "memory"); BAR(); } while (0)
#define SB() __builtin_amdgcn_sched_barrier(0)
#define PIN(p) asm volatile("" : "+v"(p))
#define RLX __ATOMIC_RELAXED, __HIP_MEMORY_SCOPE_AGENT

DEV unsigned f2u(float f) { return __builtin_bit_cast(unsigned, f); }
DEV float u2f(unsigned u) { return __builtin_bit_cast(float, u); }
DEV void dma(unsigned m0v, unsigned voff, __amdgpu_buffer_rsrc_t srd, unsigned soff) {
  asm volatile("s_mov_b32 m0, %0\n\ts_nop 0\n\tbuffer_load_dwordx4 %1, %2, %3 offen lds" ::"s"(m0v), "v"(voff), "s"(srd), "s"(soff) : "memory");
}
DEV bf16x8 ld128(LAS char* p) { return *(LAS bf16x8*)p; }
DEV bf16x8 ldtr(LAS char* p, unsigned tstep) {
  s16x4 a = __builtin_amdgcn_ds_read_tr16_b64_v4i16((LAS s16x4*)p);
  s16x4 b = __builtin_amdgcn_ds_read_tr16_b64_v4i16((LAS s16x4*)(p + tstep));
  s16x8 r = __builtin_shufflevector(a, b, 0, 1, 2, 3, 4, 5, 6, 7);
  return __builtin_bit_cast(bf16x8, r);
}
DEV unsigned pk(float a, float b) { bf16x2 r = {(__bf16)a, (__bf16)b}; return __builtin_bit_cast(unsigned, r); }
DEV f32x16 mfma(bf16x8 a, bf16x8 b, f32x16 c) { return __builtin_amdgcn_mfma_f32_32x32x16_bf16(a, b, c, 0, 0, 0); }
DEV float fmax2(float a, float b) { return __builtin_fmaxf(a, b); }
DEV float max4(f32x4 v) { return fmax2(fmax2(v.x, v.y), fmax2(v.z, v.w)); }
DEV float ex2(float a) { return __builtin_amdgcn_exp2f(a); }
DEV u32x4 pack8(const f32x16& a, int ks) {
  unsigned r0 = pk(a[8 * ks + 0], a[8 * ks + 1]), r1 = pk(a[8 * ks + 2], a[8 * ks + 3]);
  unsigned r2 = pk(a[8 * ks + 4], a[8 * ks + 5]), r3 = pk(a[8 * ks + 6], a[8 * ks + 7]);
  u32x2 s0 = __builtin_amdgcn_permlane32_swap(r0, r2, false, false);
  u32x2 s1 = __builtin_amdgcn_permlane32_swap(r1, r3, false, false);
  return u32x4{s0.x, s1.x, s0.y, s1.y};
}
DEV unsigned rot(unsigned v, int r) { return __builtin_amdgcn_alignbit(v, v, r); }
DEV unsigned csum(u32x4 a, u32x4 b, unsigned t0) {
  return (a.x + rot(a.y, 3) + rot(a.z, 7)) + (rot(a.w, 11) + rot(b.x, 13) + rot(b.y, 17)) + (rot(b.z, 23) + rot(b.w, 29) + t0);
}
template <int AUX> DEV void put(u32x4 a, u32x4 b, unsigned t0, unsigned t1, __amdgpu_buffer_rsrc_t srd, unsigned v16, unsigned v8, unsigned so, unsigned ss) {
  unsigned c = csum(a, b, t0);
  PIN(c);
  __builtin_amdgcn_raw_buffer_store_b128(a, srd, v16, so, AUX);
  __builtin_amdgcn_raw_buffer_store_b128(b, srd, v16, so + 1024, AUX);
  __builtin_amdgcn_raw_buffer_store_b64(u32x2{c, t1}, srd, v8, ss, AUX);
  asm volatile("s_nop 1" :: "v"(a), "v"(b) : "memory");
}
DEV bool none(bool c) { return __builtin_amdgcn_ballot_w64(c) == 0; }

constexpr int SLOT[72] = {0, 1, 2, 3, 4, 5, 6, 7, 8, 9, 10, 11, 12, 13, 14, 15, 16, 17, 18, 19, 0, 1, 2, 3,
                          4, 5, 6, 7, 8, 9, 10, 11, 12, 13, 14, 15, 0, 1, 2, 3,
                          4, 5, 6, 7, 8, 9, 10, 11, 12, 13, 14, 15, 0, 1, 2, 3,
                          16, 17, 18, 6, 7, 0, 1, 2, 3, 4, 5, 8, 9, 10, 11, 12};
constexpr int AVAIL[16] = {20, 23, 26, 29, 32, 35, 36, 36, 40, 44, 48, 52, 56, 56, 56, 56};
constexpr int NISS[8] = {ISS};
constexpr int NFILL[9] = {FILL};
constexpr unsigned slot(int n) { return (unsigned)SLOT[n] * H; }
constexpr int ustart(int s) { return s < 8 ? 3 * s : 24 + 4 * (s - 8); }
constexpr int uend(int s) { return ustart(s + 1); }
constexpr int cmin(int a, int b) { return a < b ? a : b; }
constexpr int cmax(int a, int b) { return a > b ? a : b; }
constexpr int issued(int s) { return s < 0 ? INITN : cmax(INITN, cmin(AVAIL[s], s < 7 ? uend(s) + CAPA : s == 7 ? M7 : NISS[s - 8])); }
constexpr int filled(int s) { return s < 7 ? 0 : NFILL[s - 7]; }
constexpr int when(int n) { int s = -1; while (issued(s) <= n) ++s; return s; }
constexpr unsigned QIM = 16 * H, SMALL = 19 * H, RX = 4 * H, RY = 8 * H, OIM = 14 * H, POIM = 13 * H;
constexpr unsigned WS_O = 0, WS_P = 6u << 20, WS_F = 16u << 20;
constexpr float C = 0.08838834764831845f * 1.4426950408889634f;

template <int V> struct IC { static constexpr int v = V; };
template <int A, int B, class F> DEV void sfor(F&& f) {
  if constexpr (A < B) { f(IC<A>{}); sfor<A + 1, B>(f); }
}

struct Ld {
  __amdgpu_buffer_rsrc_t sX, sK, sV, sWq, sWo;
  unsigned ldsb, vx0, vx1, vwq, vk, vv, vwo, xb, wqb, kvb, wob, rotA, g, hd[4];
};
template <int M> DEV void issue1(const Ld& L) {
  constexpr int N = M / 2, P = M % 2;
  const unsigned d = L.ldsb + slot(N) + P * 1024;
  if constexpr (N < 24) {
    constexpr int j = N / 3, r = N % 3;
    const unsigned jr = (j + L.rotA) & 7;
    if constexpr (r == 0) dma(d, P ? L.vx1 : L.vx0, L.sX, L.xb + jr * 128 + P * 8192);
    else dma(d, L.vwq, L.sWq, L.wqb + (64 * jr + 32 * (r - 1) + 4 * P) * 8192);
  } else {
    constexpr int u = ((N - 24) / 4) & 3, j = (N - 24) % 4;
    const unsigned so = L.kvb + (128 * ((u + L.g) & 3) + 32 * j) * 256 + P * 1024;
    if constexpr (N >= 40) dma(d, L.vv, L.sV, so);
    else dma(d, L.vk ^ (P * 64), L.sK, so);
  }
}
template <int A, int B> DEV void issue_hu(const Ld& L) { sfor<2 * A, 2 * B>([&](auto M) { issue1<M.v>(L); }); }
#define VWAIT(S) do { static_assert(issued((S) - 1) >= uend(S), "unit not issued"); \
  asm volatile("s_waitcnt vmcnt(%0)" ::"i"(2 * (issued((S) - 1) - uend(S) + filled((S) - 1) - filled(when(uend(S) - 1) - 1))) : "memory"); } while (0)

__global__ __launch_bounds__(768, 3) KATTR void gqa_fused(const void* __restrict__ x, const void* __restrict__ kc, const void* __restrict__ vc,
                                                    const void* __restrict__ wq, const void* __restrict__ wo, void* __restrict__ out, char* ws) {
  extern __shared__ __attribute__((aligned(16))) char smem[];
  LAS char* lds = (LAS char*)smem;
  const int wid = blockIdx.x, kvh = wid & 7, g = (wid >> 3) & 3, b = wid >> 5, h = kvh * 4 + g;
  const int wave = __builtin_amdgcn_readfirstlane(threadIdx.x >> 6), lane = threadIdx.x & 63;
  asm volatile("" :: "s"(x), "s"(wq), "s"(kc), "s"(vc), "s"(wo), "s"(ws), "s"(out));
  u64* epoch = (u64*)(ws + WS_F) + b * 16;
  const __amdgpu_buffer_rsrc_t sW = __builtin_amdgcn_make_buffer_rsrc(ws, 0, WS_F, 0x00020000);
  const unsigned pgrp = WS_P + (unsigned)(b * 4 + g) * 163840u;
  const int w = wave & 3, hf = lane >> 5, l32 = lane & 31, e = w & 1, kq = (wave >> 1) & 3, er = (wave >> 2) & 1;
  const unsigned q4 = (lane & 15) >> 2, p4 = lane & 3, blk = (lane >> 4) & 1;
  LAS char* lin = lds + lane * 16;
  LAS char* mxr = lds + SMALL + e * 512 + l32 * 16;
  STN();
  const int pb = b % 3;
  auto phead = [&](int p) { const int r = p + pb; return (g + 1 + (r >= 3 ? r - 3 : r)) & 3; };
  auto linit = [&](Ld& L) {
    L.sX = __builtin_amdgcn_make_buffer_rsrc((void*)x, 0, 8u * 64 * 512 * 2, 0x00020000);
    L.sWq = __builtin_amdgcn_make_buffer_rsrc((void*)wq, 0, 4u << 20, 0x00020000);
    L.ldsb = (unsigned)(size_t)lds + w * 2048;
    const unsigned r4 = lane >> 4, c16 = lane & 15, swT = (c16 ^ (r4 << 2)) * 16;
    L.vx0 = (lane >> 3) * 1024 + (((lane & 7) ^ r4) * 16);
    L.vx1 = (lane >> 3) * 1024 + (((lane & 7) ^ (4 + r4)) * 16);
    L.vwq = r4 * 8192 + swT;
    L.xb = b * 65536 + w * 16 * 1024;
    L.wqb = h * 256 + w * 8 * 8192;
    L.rotA = b + 2 * g; L.g = g;
  };
  int wv2 = wave;
  u64 ep = 0;
  if (wave == 0) ep = __hip_atomic_load(epoch, RLX);
  if (wave >= 8) {
    Ld L;
    if (LPRIO) __builtin_amdgcn_s_setprio(LPRIO);
    linit(L);
    issue_hu<0, 3>(L);
    __builtin_amdgcn_s_sleep(ISLP);
    issue_hu<3, 6>(L);
    __builtin_amdgcn_s_sleep(ISLP);
    issue_hu<6, INITN>(L);
    if (LP1 != LPRIO) __builtin_amdgcn_s_setprio(LP1);
  }
  asm volatile("" : "+s"(wv2));
  if (wv2 >= 8) {
    Ld L;
    linit(L);
    const unsigned r4 = lane >> 4, c16 = lane & 15, swT = (c16 ^ (r4 << 2)) * 16;
    L.sK = __builtin_amdgcn_make_buffer_rsrc((void*)kc, 0, 8u << 20, 0x00020000);
    L.sV = __builtin_amdgcn_make_buffer_rsrc((void*)vc, 0, 8u << 20, 0x00020000);
    L.sWo = __builtin_amdgcn_make_buffer_rsrc((void*)wo, 0, 4u << 20, 0x00020000);
    L.vv = r4 * 256 + swT; L.vwo = r4 * 1024 + swT;
    L.vk = r4 * 256 + ((c16 ^ (8 * (w & 1) + r4)) * 16);
    L.kvb = (unsigned)(b * 8 + kvh) * 131072u + w * 2048;
    L.wob = kvh * 524288 + g * 256 + w * 8 * 1024;
    L.hd[0] = L.wob + g * 131072;
#pragma unroll
    for (int p = 0; p < 3; ++p) L.hd[p + 1] = L.wob + phead(p) * 131072;
    u32x4 wr[32];
    LAS char* wd[3];
    wd[0] = lin + w * 2048; wd[1] = wd[0] + 8 * H; wd[2] = wd[0] + 16 * H;
    PIN(wd[0]); PIN(wd[1]); PIN(wd[2]);
    auto wput = [&](auto A, auto B) {
      sfor<A.v, B.v>([&](auto M) {
        constexpr int sl = SLOT[56 + M.v / 2];
        *(LAS u32x4*)(wd[sl >> 3] + (sl & 7) * H + (M.v & 1) * 1024) = wr[M.v];
      });
    };
    sfor<0, 16>([&](auto S) {
      VWAIT(S.v);
      if constexpr (S.v == 15) wput(IC<0>{}, IC<G1>{});
      BAR();
      issue_hu<issued(S.v - 1), issued(S.v)>(L);
      sfor<2 * filled(S.v - 1), 2 * filled(S.v)>([&](auto M) {
        wr[M.v] = __builtin_amdgcn_raw_buffer_load_b128(L.sWo, L.vwo, L.hd[M.v >> 3] + (32 * ((M.v >> 1) & 3) + 4 * (M.v & 1)) * 1024, 0);
      });
      asm volatile("" ::: "memory");
    });
    if (LPRIO) __builtin_amdgcn_s_setprio(0);
    wput(IC<G1>{}, IC<G1B>{});
    BAR_C();
    wput(IC<G1B>{}, IC<G2>{});
    BAR_C();
    if (LP2) __builtin_amdgcn_s_setprio(LP2);
    wput(IC<G2>{}, IC<32>{});
    BAR_C();
    return;
  }
  bf16x8 qf[8];
  f32x16 O[4];
  float mloc, inv;
  f32x4 fk, mv, lv;
  auto factors2 = [&]() {
    const float m = max4(mv);
    fk = f32x4{ex2(mv.x - m), ex2(mv.y - m), ex2(mv.z - m), ex2(mv.w - m)};
  };
  const unsigned trb = 256 * (4 * hf + q4) + 8 * (p4 & 1), trx = 2 * blk + (p4 >> 1);
  auto attn = [&](auto ZC) {
    constexpr int Z = ZC.v;
    auto bar = [&](auto R, bool drain) {
      if (drain) BAR_C(); else BAR();
    };
    LAS char* ka[8];
#pragma unroll
    for (int ks = 0; ks < 8; ++ks) { ka[ks] = lds + slot(24 + 8 * Z) + (w >> 1) * 2 * H + 256 * l32 + 16 * ((2 * ks + hf) ^ (l32 & 15)); PIN(ka[ks]); }
    bf16x8 kf[32];
    f32x16 S[4];
    float mt[4];
    auto tmax = [&](const f32x16& a) {
      float m = fmax2(fmax2(a[0], a[1]), a[2]);
#pragma unroll
      for (int r = 3; r < 15; r += 2) m = fmax2(fmax2(m, a[r]), a[r + 1]);
      return fmax2(m, a[15]);
    };
    sfor<0, 36>([&](auto T) {
      constexpr int t = T.v;
      if constexpr (t == 0 && Z) bar(IC<0>{}, false);
      if constexpr (t == 16) bar(IC<1>{}, false);
      if constexpr (t == 30) bar(IC<2>{}, false);
      if constexpr ((t & 15) < 8 && t < 32) {
        constexpr int i = (t & 16) + 2 * (t & 7);
        kf[i] = ld128(ka[i & 7] + (Z ? 0 : (i >> 4) * 4 * H) + ((i >> 3) & 1) * H);
        kf[i + 1] = ld128(ka[(i + 1) & 7] + (Z ? 0 : (i >> 4) * 4 * H) + ((i >> 3) & 1) * H);
      }
      if constexpr (Z && t >= 8 && t < 16) { ka[t - 8] -= slot(32) - slot(36); PIN(ka[t - 8]); }
      if constexpr (!Z && t < 4) { qf[2 * t] = ld128(lin + QIM + e * H + 2 * t * 1024); qf[2 * t + 1] = ld128(lin + QIM + e * H + (2 * t + 1) * 1024); }
      if constexpr (t >= 4) S[(t - 4) >> 3] = mfma(kf[t - 4], qf[(t - 4) & 7], ((t - 4) & 7) == 0 ? f32x16{} : S[(t - 4) >> 3]);
      if constexpr (t == 15 || t == 23 || t == 31) mt[(t - 15) >> 3] = tmax(S[(t - 15) >> 3]);
      SB();
    });
    {
      const float m = fmax2(fmax2(mt[0], mt[1]), fmax2(mt[2], tmax(S[3])));
      u32x2 sw = __builtin_amdgcn_permlane32_swap(f2u(m), f2u(m), false, false);
      mloc = fmax2(u2f(sw.x), u2f(sw.y));
      if (hf == 0) *(LAS float*)(mxr + kq * 4) = mloc;
    }
    {
      const unsigned hi = f2u(-mloc) & 0xffff0000u;
      const float r1 = -mloc - u2f(hi);
      const unsigned mid = f2u(r1) & 0xffff0000u;
      const float lo = r1 - u2f(mid);
      const bf16x8 mb = __builtin_bit_cast(bf16x8, u32x4{mid | (hi >> 16), f2u(lo) >> 16, 0u, 0u});
      const bf16x8 ma = __builtin_bit_cast(bf16x8, u32x4{hf ? 0u : 0x3f803f80u, hf ? 0u : 0x3f80u, 0u, 0u});
#pragma unroll
      for (int i = 0; i < 4; ++i) S[i] = mfma(ma, mb, S[i]);
      SB();
    }
    unsigned pp[32];
    f32x4 ls = {};
    const unsigned one = ((lane >> 4) ^ lane) & 1 ? 0u : 0x3f803f80u;
    const bf16x8 sel = __builtin_bit_cast(bf16x8, u32x4{one, one, one, one});
    LAS char* va[4];
    sfor<0, 33>([&](auto T) {
      constexpr int t = T.v;
      if constexpr (t == (Z ? E3Y : E3)) bar(IC<3>{}, true);
      if constexpr (t < 32) {
        pp[t] = pk(ex2(S[t >> 3][2 * (t & 7)]), ex2(S[t >> 3][2 * (t & 7) + 1]));
      }
      if constexpr (t >= 4 && (t & 3) == 0)
        ls = __builtin_amdgcn_mfma_f32_16x16x32_bf16(sel, __builtin_bit_cast(bf16x8, u32x4{pp[t - 4], pp[t - 3], pp[t - 2], pp[t - 1]}), ls, 0, 0, 0);
      if constexpr (t >= 28 && t < 32) { va[t - 28] = lds + slot(40 + 8 * Z) + (w >> 1) * 2 * H + trb + 16 * ((4 * ((kq + t - 27) & 3) + trx) ^ (q4 << 2)); PIN(va[t - 28]); }
      SB();
    });
    bf16x8 vf[32];
    LAS char* ow = Z ? lin + RY + (e * 6 + (kq & 1) * 3) * 4096 : lin + RX + e * 4096;
    auto put_tile = [&](LAS char* p, const f32x16& o) {
#pragma unroll
      for (int q = 0; q < 4; ++q) *(LAS f32x4*)(p + q * 1024) = f32x4{o[4 * q], o[4 * q + 1], o[4 * q + 2], o[4 * q + 3]};
    };
    sfor<0, 38>([&](auto T) {
      constexpr int t = T.v;
      constexpr auto kt = [](int i) { return i < 16 ? i >> 3 : 2 + ((i >> 1) & 1); };
      constexpr auto db = [](int i) { return i < 16 ? (i >> 1) & 3 : (i >> 2) & 3; };
      if constexpr (t == 0) bar(IC<4>{}, false);
      if constexpr (t == 16) bar(IC<5>{}, false);
      if constexpr (t < 32) vf[t] = ldtr(va[db(t)] + (Z ? 0 : (t >> 4) * 4 * H) + (kt(t) & 1) * H + (t & 1) * 4096, 2048);
      if constexpr (Z && t >= 9 && t < 16 && (t & 1)) { va[(t >> 1) & 3] -= slot(48) - slot(52); PIN(va[(t >> 1) & 3]); }
      if constexpr (t >= 4 && t < 36) {
        constexpr int i = t - 4, p = kt(i) * 8 + (i & 1) * 4;
        const u32x4 pv = {pp[p], pp[p + 1], pp[p + 2], pp[p + 3]};
        O[db(i)] = mfma(vf[i], __builtin_bit_cast(bf16x8, pv), i < 8 && !(i & 1) ? f32x16{} : O[db(i)]);
      }
      if constexpr (t == 2) { if (hf == 0) *(LAS float*)(mxr + 1024 + kq * 4) = blk ? ls.y : ls.x; }
      if constexpr (t >= PT && t < PT + 12) {
        constexpr int j = (t - PT) >> 2, q = (t - PT) & 3;
        const f32x4 v = {O[j][4 * q], O[j][4 * q + 1], O[j][4 * q + 2], O[j][4 * q + 3]};
        if constexpr (Z) *(LAS f32x4*)(ow + j * 4096 + q * 1024) = v;
        else if constexpr (j != 1) *(LAS f32x4*)(ow + (((kq + 1 + j) & 3) ^ 2) * 8192 + q * 1024) = v;
      }
      if constexpr (Z && EARLYF) {
        if constexpr (t == 8) mv = *(LAS f32x4*)mxr;
        if constexpr (t == 12) factors2();
        if constexpr (t == 17) lv = *(LAS f32x4*)(mxr + 1024);
        if constexpr (t == 21) { inv = __builtin_amdgcn_rcpf((lv.x * fk.x + lv.y * fk.y) + (lv.z * fk.z + lv.w * fk.w)); PIN(inv); }
      }
      SB();
    });
  };
  auto get_tile = [&](LAS char* p, f32x4* v) {
#pragma unroll
    for (int q = 0; q < 4; ++q) v[q] = *(LAS f32x4*)(p + q * 1024);
  };
  f32x4 ta[4], tb[4];
  auto factors = [&]() { mv = *(LAS f32x4*)mxr; factors2(); };
  float fo, fa, fb;
  u32x2 tag;
  auto rinv = [&]() {
    lv = *(LAS f32x4*)(mxr + 1024);
    tag = *(LAS u32x2*)(lds + SMALL + 2048);
    return __builtin_amdgcn_rcpf((lv.x * fk.x + lv.y * fk.y) + (lv.z * fk.z + lv.w * fk.w));
  };
  {
    LAS char *trN[3], *xa[3][4];
#pragma unroll
    for (int r = 0; r < 3; ++r) {
      trN[r] = lds + r * 8 * H + 256 * (8 * hf + q4) + 16 * ((4 * w + trx) ^ (q4 << 2)) + 8 * (p4 & 1); PIN(trN[r]);
#pragma unroll
      for (int ks = 0; ks < 4; ++ks) { xa[r][ks] = lds + r * 8 * H + er * 4096 + 128 * l32 + 16 * ((2 * ks + hf) ^ ((l32 >> 1) & 7)); PIN(xa[r][ks]); }
    }
    f32x16 acc = {};
    bf16x8 fa_[4], fb_[4];
    sfor<0, 35>([&](auto T) {
      constexpr int t = T.v;
      if constexpr (t < 32) {
        constexpr int j = t >> 2, ks = t & 3, sa = SLOT[3 * j + 1 + (ks >> 1)], sx = SLOT[3 * j];
        if constexpr (ks == 0) BAR();
        fa_[t & 3] = ldtr(trN[sa >> 3] + (sa & 7) * H + (ks & 1) * 4096, 1024);
        fb_[t & 3] = ld128(xa[sx >> 3][ks] + (sx & 7) * H);
      }
      if constexpr (t >= 3) acc = mfma(fa_[(t - 3) & 3], fb_[(t - 3) & 3], acc);
      if constexpr (t == 30) {
        if (wave == 0) {
          PIN(ep);
          if (lane == 0) *(LAS u64*)(lds + SMALL + 2048) = ep * 0x9E3779B97F4A7C15ull + 0x632BE59BD9B4E019ull;
        }
      }
      SB();
    });
#pragma unroll
    for (int k = 0; k < 16; ++k) acc[k] *= C;
#pragma unroll
    for (int ks = 0; ks < 2; ++ks) *(LAS u32x4*)(lin + QIM + er * H + (2 * w + ks) * 1024) = pack8(acc, ks);
    BAR_C();
  }
  if (wave >= 4) {
#pragma unroll
    for (int k = 0; k < 8; ++k) qf[k] = ld128(lin + QIM + e * H + k * 1024);
    BAR();
    attn(IC<1>{});
    if (!EARLYF) factors();
    get_tile(lin + RX + ((kq ^ 2) * 2 + e) * 4096, ta);
    if (EARLYF) tag = *(LAS u32x2*)(lds + SMALL + 2048); else inv = rinv();
    fo = ((kq & 1) ? fk.w : fk.z) * inv; fb = ((kq & 1) ? fk.z : fk.w) * inv;
#pragma unroll
    for (int k = 0; k < 16; ++k) O[3][k] = __builtin_fmaf(ta[k >> 2][k & 3], inv, O[3][k] * fo);
    SB();
    BAR_C();
    get_tile(lin + RY + (e * 6 + ((kq & 1) ^ 1) * 3 + ((kq & 1) ? 0 : 2)) * 4096, tb);
  } else {
    attn(IC<0>{});
    BAR_C();
    factors();
    LAS char* r1 = lin + RX + (kq * 2 + e) * 4096;
    get_tile(r1, ta);
    get_tile(lin + RX + ((kq ^ 2) * 2 + e) * 4096, tb);
    {
      const float fs = kq ? fk.y : fk.x, fr = kq ? fk.x : fk.y;
#pragma unroll
      for (int q = 0; q < 4; ++q) {
        f32x4 v;
#pragma unroll
        for (int k = 0; k < 4; ++k) v[k] = __builtin_fmaf(ta[q][k], fr, O[1][4 * q + k] * fs);
        *(LAS f32x4*)(r1 + q * 1024) = v;
      }
#pragma unroll
      for (int k = 0; k < 16; ++k) O[3][k] = __builtin_fmaf(tb[k >> 2][k & 3], fr, O[3][k] * fs);
    }
    BAR_C();
    inv = rinv();
    fa = fk.z * inv; fb = fk.w * inv;
#pragma unroll
    for (int k = 0; k < 16; ++k) O[3][k] *= inv;
    SB();
    BAR();
    get_tile(lin + RY + (e * 6 + ((kq + 1) & 3)) * 4096, ta);
    get_tile(lin + RY + (e * 6 + 3 + kq) * 4096, tb);
#pragma unroll
    for (int k = 0; k < 16; ++k) O[3][k] = __builtin_fmaf(ta[k >> 2][k & 3], fa, O[3][k]);
  }
  const unsigned t0 = tag.x, t1 = tag.y;
  {
    u32x4 o[2];
#pragma unroll
    for (int k = 0; k < 8; ++k) {
      const int r = 2 * k;
      o[k >> 2][k & 3] = pk(__builtin_fmaf(tb[r >> 2][r & 3], fb, O[3][r]), __builtin_fmaf(tb[r >> 2][(r & 3) + 1], fb, O[3][r + 1]));
    }
    const unsigned so = WS_O + (unsigned)wid * 20480u + wave * 2560;
    put<SA1>(o[0], o[1], t0, t1, sW, lane * 16, lane * 8, so, so + 2048);
    *(LAS u32x4*)(lin + OIM + (kq * 4 + e) * 1024) = o[0];
    *(LAS u32x4*)(lin + OIM + (kq * 4 + 2 + e) * 1024) = o[1];
  }
  BAR_C();
  f32x16 dd;
  {
    LAS char* wa[3];
    wa[0] = lds + trb + 16 * ((4 * w + trx) ^ (q4 << 2)); wa[1] = wa[0] + 8 * H; wa[2] = wa[0] + 16 * H;
    LAS char* ob = lin + POIM + er * 1024;
    PIN(wa[0]); PIN(wa[1]); PIN(wa[2]); PIN(ob);
    bf16x8 fa_[4], fb_[4];
    u32x4 x0[3], x1[3]; u32x2 xs[3];
    unsigned pso[3];
#pragma unroll
    for (int p = 0; p < 3; ++p) pso[p] = WS_O + (unsigned)((wid & ~24) | (phead(p) << 3)) * 20480u + wave * 2560;
    auto poll = [&](int p) { xs[p] = __builtin_amdgcn_raw_buffer_load_b64(sW, lane * 8, pso[p] + 2048, LA1); };
    auto fetch = [&](int p) {
      x0[p] = __builtin_amdgcn_raw_buffer_load_b128(sW, lane * 16, pso[p], LA1);
      x1[p] = __builtin_amdgcn_raw_buffer_load_b128(sW, lane * 16, pso[p] + 1024, LA1);
    };
    auto poll3 = [&]() { asm volatile("" ::: "memory"); poll(0); poll(1); poll(2); };
    auto fetch3 = [&]() { asm volatile("" ::: "memory"); fetch(0); fetch(1); fetch(2); };
    auto fresh = [&]() { return none(xs[0].y != t1 || xs[1].y != t1 || xs[2].y != t1); };
    auto lda = [&](auto K) { constexpr int k = K.v; return ldtr(wa[SLOT[56 + (k >> 1)] >> 3] + (SLOT[56 + (k >> 1)] & 7) * H + (k & 1) * 4096, 2048); };
    sfor<0, 11>([&](auto T) {
      constexpr int t = T.v;
      if constexpr (t < 8) {
        fa_[t & 3] = lda(T);
        fb_[t & 3] = ld128(ob + (OIM - POIM) + t * 2048);
      }
      if constexpr (t >= 3) dd = mfma(fa_[(t - 3) & 3], fb_[(t - 3) & 3], t == 3 ? f32x16{} : dd);
      SB();
    });
    auto all3 = [&]() { asm volatile("" ::: "memory"); fetch(0); poll(0); fetch(1); poll(1); fetch(2); poll(2); };
    BAR_C();
    bf16x8 fA[24];
    sfor<0, NPRE>([&](auto K) {
      if constexpr (FS == 0 && K.v == DL1) { SB(); all3(); SB(); }
      if constexpr (FS && (K.v == DL1 || K.v == DL1 + FS || K.v == DL1 + 2 * FS)) {
        constexpr int p = (K.v - DL1) / FS;
        SB();
        if constexpr (p > 0 && FSB) asm volatile("s_barrier" ::: "memory");
        asm volatile("" ::: "memory");
        fetch(p); poll(p);
        SB();
      }
      fA[K.v] = lda(IC<K.v + 8>{});
    });
    SB();
    STN();
    auto land = [&](auto P) {
      constexpr int p = P.v;
      LAS char* dst = lin + POIM + p * 2 * H + (kq * 4 + e) * 1024;
      if (none(csum(x0[p], x1[p], t0) != xs[p].x || xs[p].y != t1)) {
        *(LAS u32x4*)dst = x0[p];
        *(LAS u32x4*)(dst + 2048) = x1[p];
      } else {
        u32x4 y0, y1; u32x2 ys;
        for (int it = 0; it < (1 << 8); ++it) {
          ys = xs[p];
          for (int i2 = 0; i2 < (1 << 12) && !none(ys.y != t1); ++i2) {
            if (SLP) __builtin_amdgcn_s_sleep(SLP);
            asm volatile("" ::: "memory");
            ys = __builtin_amdgcn_raw_buffer_load_b64(sW, lane * 8, pso[p] + 2048, LA1);
          }
          xs[p].y = ~t1;
          asm volatile("" ::: "memory");
          y0 = __builtin_amdgcn_raw_buffer_load_b128(sW, lane * 16, pso[p], LA1);
          y1 = __builtin_amdgcn_raw_buffer_load_b128(sW, lane * 16, pso[p] + 1024, LA1);
          if (none(csum(y0, y1, t0) != ys.x || ys.y != t1)) break;
        }
        *(LAS u32x4*)dst = y0;
        *(LAS u32x4*)(dst + 2048) = y1;
        asm volatile("" ::: "memory");
      }
    };
    bf16x8 fB[LAG + 1];
    sfor<0, 24 + LAG>([&](auto T) {
      constexpr int t = T.v;
      if constexpr (SPLIT && t < 24 && (t & 7) == 0) { land(IC<t / 8>{}); BAR_C(); }
      if constexpr (!SPLIT && t == 0) { land(IC<0>{}); land(IC<1>{}); land(IC<2>{}); BAR_C(); }
      if constexpr (t < 24) fB[t % (LAG + 1)] = ld128(ob + (t >> 3) * 2 * H + (t & 7) * 2048);
      if constexpr (t > LAG && t - LAG - 1 + NPRE < 24) fA[t - LAG - 1 + NPRE] = lda(IC<t - LAG + 7 + NPRE>{});
      if constexpr (t >= LAG) dd = mfma(fA[t - LAG], fB[(t - LAG) % (LAG + 1)], dd);
      SB();
    });
  }
  STN();
  {
    const unsigned vo = (l32 >> 3) * 20480 + ((l32 >> 2) & 1) * 128 + (hf * 4 + (l32 & 3)) * 16;
    const unsigned vc2 = (l32 >> 3) * 20480 + ((l32 >> 2) & 1) * 64 + (hf * 4 + (l32 & 3)) * 8;
    const unsigned so = pgrp + er * 81920 + kvh * 2560;
    const u32x4 a = pack8(dd, 0), c = pack8(dd, 1);
    put<SA2>(a, c, t0, t1, sW, vo, vc2, so + w * 256, so + 2048 + w * 128);
  }
  STN();
  {
    const unsigned vo = (lane >> 3) * 2560 + (lane & 7) * 16, vc2 = (lane >> 3) * 2560 + (lane & 7) * 8;
    const unsigned so = pgrp + kvh * 20480;
    u32x4 v0, v1; u32x2 cs;
    asm volatile("s_waitcnt vmcnt(0)" ::: "memory");
    STN();
    if (DL2) __builtin_amdgcn_s_sleep(DL2);
    bool bad = true;
    for (int it = 0; it < (1 << 20); ++it) {
      if (!MASKP || bad) {
        v0 = __builtin_amdgcn_raw_buffer_load_b128(sW, vo, so + (w * 2 + er) * 128, LA2);
        v1 = __builtin_amdgcn_raw_buffer_load_b128(sW, vo, so + 1024 + (w * 2 + er) * 128, LA2);
        cs = __builtin_amdgcn_raw_buffer_load_b64(sW, vc2, so + 2048 + (w * 2 + er) * 64, LA2);
      }
      bad = csum(v0, v1, t0) != cs.x || cs.y != t1;
      if (none(bad)) break;
      if (SL4) __builtin_amdgcn_s_sleep(SL4);
      asm volatile("" ::: "memory");
    }
    if ((wid & 31) == 0 && wave == 0 && lane == 0) __hip_atomic_store(epoch, __builtin_bit_cast(u64, tag), RLX);
    float c8[8], c4[4];
#pragma unroll
    for (int k = 0; k < 8; ++k) {
      const unsigned a0 = v0[k >> 1], b0 = v1[k >> 1];
      u32x2 sw = __builtin_amdgcn_permlane32_swap((k & 1) ? a0 & 0xffff0000u : a0 << 16, (k & 1) ? b0 & 0xffff0000u : b0 << 16, false, false);
      c8[k] = u2f(sw.x) + u2f(sw.y);
    }
#pragma unroll
    for (int k = 0; k < 4; ++k) {
      u32x2 sw = __builtin_amdgcn_permlane16_swap(f2u(c8[k]), f2u(c8[k + 4]), false, false);
      const float s = u2f(sw.x) + u2f(sw.y);
      c4[k] = s + u2f(__builtin_amdgcn_update_dpp(0, f2u(s), 0x128, 0xf, 0xf, false));
    }
    const bool up = lane & 8;
    const unsigned s = 8 * kvh + 4 * er + (lane & 3);
    const unsigned n = 128 * g + 32 * w + 16 * hf + 8 * ((lane >> 2) & 1) + 4 * ((lane >> 4) & 1) + 2 * ((lane >> 3) & 1);
    *(unsigned*)((char*)out + ((size_t)(b * 64 + s) * 512 + n) * 2) = pk(up ? c4[2] : c4[0], up ? c4[3] : c4[1]);
  }
}

#ifdef __HIP_DEVICE_COMPILE__
#pragma clang attribute pop
#endif
extern "C" void kernel_launch(void* const* d_in, const int* in_sizes, int n_in, void* d_out, int out_size, void* d_ws, size_t ws_size,
                              hipStream_t stream) {
  (void)hipFuncSetAttribute((const void*)gqa_fused, hipFuncAttributeMaxDynamicSharedMemorySize, 163840);
  gqa_fused<<<256, 768, 163840, stream>>>(d_in[0], d_in[1], d_in[2], d_in[3], d_in[4], d_out, (char*)d_ws);
}
```
